```python
import torch

def _softmax(x, axis=-1):
    return torch.softmax(x, dim=axis)
def _gelu(x):
    return torch.nn.functional.gelu(x, approximate="tanh")
def _silu(x):
    return torch.nn.functional.silu(x)
def _sigmoid(x):
    return torch.sigmoid(x)
def _layernorm(x, axis=-1, eps=1e-5):
    m = x.mean(dim=axis, keepdim=True)
    v = x.var(dim=axis, keepdim=True, unbiased=False)
    return (x - m) / torch.sqrt(v + eps)
def _scatter_add(tgt, idx, src, axis=0):
    return tgt.clone().index_add_(axis, idx, src)
def _segment_sum(src, seg, n):
    out = torch.zeros((n,) + src.shape[1:], dtype=src.dtype)
    return out.index_add_(0, seg, src)
def _pad(x, pw):
    flat = tuple(v for pair in reversed(pw) for v in pair)
    return torch.nn.functional.pad(x, flat)

kernel_name = 'gated_mix__gqa_block_cached__glu_gated_glu__decode'

def setup_inputs():

    _w = (lambda a: (lambda t: t.to(torch.bfloat16) if t.is_floating_point() else t)(torch.as_tensor(a)))
    def _A_setup():
        B, Sq, D = 4, 1, 512
        Hq, Hkv, Sk, Dh = 32, 8, 8192, 128
        assert Hq % Hkv == 0, (Hq, Hkv)
        return {
            "x": _w((lambda *s: torch.randn(*s, dtype=torch.bfloat16) * 0.05)(B, Sq, D)),
            "k_cache": _w((lambda *s: torch.randn(*s, dtype=torch.bfloat16) * 6.4)(B, Hkv, Sk, Dh)),
            "v_cache": _w((lambda *s: torch.randn(*s, dtype=torch.bfloat16) * 3.2)(B, Hkv, Sk, Dh)),
            "wq": _w((lambda *s: torch.randn(*s, dtype=torch.bfloat16) * 6.4)(D, Hq * Dh) * (D ** -0.5)),
            "wo": _w((lambda *s: torch.randn(*s, dtype=torch.bfloat16) * 3.2)(Hq * Dh, D) * ((Hq * Dh) ** -0.5)),
        }
    _A_in = _A_setup()

    def _B_setup():
        B, S, D, F = 4, 1, 512, 1792
        return {
            "x": _w((lambda *s: torch.randn(*s, dtype=torch.bfloat16) * 0.05)(B, S, D)),
            "w_gate": _w((lambda *s: torch.randn(*s, dtype=torch.bfloat16) * 3.2)(D, F)),
            "w_up": _w((lambda *s: torch.randn(*s, dtype=torch.bfloat16) * 0.2)(D, F)),
            "w_down": _w((lambda *s: torch.randn(*s, dtype=torch.bfloat16) * 0.2)(F, D)),
        }
    _B_in = _B_setup()

    return {
        "A_x": _A_in["x"],
        "A_k_cache": _A_in["k_cache"],
        "A_v_cache": _A_in["v_cache"],
        "A_wq": _A_in["wq"],
        "A_wo": _A_in["wo"],
        "B_x": _B_in["x"],
        "B_w_gate": _B_in["w_gate"],
        "B_w_up": _B_in["w_up"],
        "B_w_down": _B_in["w_down"],
        "gate_w": _w((lambda *s: torch.randn(*s, dtype=torch.bfloat16) * 0.05)(512) * 16.0),
    }

def reference(A_x, A_k_cache, A_v_cache, A_wq, A_wo, B_x, B_w_gate, B_w_up, B_w_down, gate_w):
    def _A_ref(x, k_cache, v_cache, wq, wo):
        B, Sq, D = x.shape
        Hq, Dh = 32, 128
        Hkv = k_cache.shape[1]
        rep = Hq // Hkv
        q = (lambda x, *d: x.permute(*d))((x @ wq).reshape(B, Sq, Hq, Dh), 0, 2, 1, 3)
        k = (lambda x, n, axis: torch.repeat_interleave(x, n, dim=axis))(k_cache, rep, axis=1)
        v = (lambda x, n, axis: torch.repeat_interleave(x, n, dim=axis))(v_cache, rep, axis=1)
        scores = torch.einsum("bhqd,bhkd->bhqk", q, k) * (Dh ** -0.5)
        p = _softmax(scores, axis=-1)
        o = torch.einsum("bhqk,bhkd->bhqd", p, v)
        return (lambda x, *d: x.permute(*d))(o, 0, 2, 1, 3).reshape(B, Sq, Hq * Dh) @ wo

    def _B_ref(x, w_gate, w_up, w_down):
        g = torch.einsum("bsd,df->bsf", x, w_gate)
        u = torch.einsum("bsd,df->bsf", x, w_up)
        h = _sigmoid(g) * u
        return torch.einsum("bsf,fd->bsd", h, w_down)

    x = A_x
    g = _sigmoid((x * gate_w).sum(-1, keepdims=True))
    a = _A_ref(x, A_k_cache, A_v_cache, A_wq, A_wo)
    b = _B_ref(x, B_w_gate, B_w_up, B_w_down)
    return g * a + (1 - g) * b
```

```cpp
#include <hip/hip_runtime.h>
#include <hip/hip_bf16.h>
#include <cstdint>

typedef unsigned short u16;
__device__ __forceinline__ float bf2f(u16 b) { return __uint_as_float(((unsigned)b) << 16); }
__device__ __forceinline__ u16 f2bf(float f) {
  unsigned u = __float_as_uint(f);
  u += 0x7FFFu + ((u >> 16) & 1u);
  return (u16)(u >> 16);
}

constexpr int NB = 4, DM = 512, HQ = 32, HKV = 8, SK = 8192, DH = 128, FF = 1792;
constexpr int NSPLIT = 8;

__global__ void k_proj(const u16* x, const u16* wq, const u16* wg, const u16* wu, const u16* gw,
                       float* q, float* h, float* g) {
  int n = blockIdx.x * blockDim.x + threadIdx.x;
  if (n < HQ * DH) {
    float a[4] = {0, 0, 0, 0};
    for (int k = 0; k < DM; ++k) {
      float w = bf2f(wq[(size_t)k * (HQ * DH) + n]);
      for (int b = 0; b < 4; ++b) a[b] += bf2f(x[b * DM + k]) * w;
    }
    for (int b = 0; b < 4; ++b) q[b * HQ * DH + n] = bf2f(f2bf(a[b]));
  } else if (n < HQ * DH + FF) {
    int f = n - HQ * DH;
    float a[4] = {0, 0, 0, 0}, u[4] = {0, 0, 0, 0};
    for (int k = 0; k < DM; ++k) {
      float w1 = bf2f(wg[(size_t)k * FF + f]), w2 = bf2f(wu[(size_t)k * FF + f]);
      for (int b = 0; b < 4; ++b) { float xv = bf2f(x[b * DM + k]); a[b] += xv * w1; u[b] += xv * w2; }
    }
    for (int b = 0; b < 4; ++b) h[b * FF + f] = u[b] / (1.f + __expf(-a[b]));
  } else if (n < HQ * DH + FF + 4) {
    int b = n - HQ * DH - FF;
    float s = 0;
    for (int k = 0; k < DM; ++k) s += bf2f(x[b * DM + k]) * bf2f(gw[k]);
    g[b] = 1.f / (1.f + __expf(-s));
  }
}

__global__ void __launch_bounds__(256) k_attn(const u16* kc, const u16* vc, const float* q, float* pm, float* pl, float* po) {
  int s = blockIdx.x, kvh = blockIdx.y, b = blockIdx.z;
  __shared__ float qs[4][DH];
  __shared__ float sc[4][SK / NSPLIT];
  __shared__ float red[4][256];
  int tid = threadIdx.x;
  for (int i = tid; i < 4 * DH; i += 256) qs[i / DH][i % DH] = q[b * HQ * DH + kvh * 4 * DH + i];
  __syncthreads();
  const int NK = SK / NSPLIT;
  const u16* kb = kc + ((size_t)(b * HKV + kvh) * SK + s * NK) * DH;
  const u16* vb = vc + ((size_t)(b * HKV + kvh) * SK + s * NK) * DH;
  float mx[4] = {-INFINITY, -INFINITY, -INFINITY, -INFINITY};
  for (int k = tid; k < NK; k += 256) {
    float a[4] = {0, 0, 0, 0};
    for (int d = 0; d < DH; ++d) {
      float kv = bf2f(kb[(size_t)k * DH + d]);
      for (int h = 0; h < 4; ++h) a[h] += kv * qs[h][d];
    }
    for (int h = 0; h < 4; ++h) { a[h] *= 0.08838834764831845f; sc[h][k] = a[h]; mx[h] = fmaxf(mx[h], a[h]); }
  }
  for (int h = 0; h < 4; ++h) red[h][tid] = mx[h];
  __syncthreads();
  for (int h = 0; h < 4; ++h) { float m = -INFINITY; for (int i = 0; i < 256; ++i) m = fmaxf(m, red[h][i]); mx[h] = m; }
  __syncthreads();
  float sm[4] = {0, 0, 0, 0};
  for (int k = tid; k < NK; k += 256)
    for (int h = 0; h < 4; ++h) { float p = __expf(sc[h][k] - mx[h]); sc[h][k] = p; sm[h] += p; }
  for (int h = 0; h < 4; ++h) red[h][tid] = sm[h];
  __syncthreads();
  for (int h = 0; h < 4; ++h) { float m = 0; for (int i = 0; i < 256; ++i) m += red[h][i]; sm[h] = m; }
  int d = tid & 127, hh = tid >> 7;
  float o0 = 0, o1 = 0;
  for (int k = 0; k < NK; ++k) {
    float v = bf2f(vb[(size_t)k * DH + d]);
    o0 += sc[hh * 2][k] * v; o1 += sc[hh * 2 + 1][k] * v;
  }
  size_t base = ((size_t)(b * HQ + kvh * 4) * NSPLIT);
  po[((size_t)(b * HQ + kvh * 4 + hh * 2) * NSPLIT + s) * DH + d] = o0;
  po[((size_t)(b * HQ + kvh * 4 + hh * 2 + 1) * NSPLIT + s) * DH + d] = o1;
  if (tid < 4) { pm[base + tid * NSPLIT + s] = mx[tid]; pl[base + tid * NSPLIT + s] = sm[tid]; }
}

__global__ void k_comb(const float* pm, const float* pl, const float* po, float* o) {
  int row = blockIdx.x;
  int d = threadIdx.x;
  float M = -INFINITY;
  for (int s = 0; s < NSPLIT; ++s) M = fmaxf(M, pm[row * NSPLIT + s]);
  float L = 0, a = 0;
  for (int s = 0; s < NSPLIT; ++s) {
    float w = __expf(pm[row * NSPLIT + s] - M);
    L += w * pl[row * NSPLIT + s];
    a += w * po[((size_t)row * NSPLIT + s) * DH + d];
  }
  o[row * DH + d] = a / L;
}

__global__ void __launch_bounds__(256) k_out(const float* o, const float* h, const float* g, const u16* wo, const u16* wd, u16* out) {
  int b = blockIdx.y, c = blockIdx.x * 64 + (threadIdx.x & 63), ks = threadIdx.x >> 6;
  float a = 0, bb = 0;
  for (int k = ks; k < HQ * DH; k += 4) a += o[b * HQ * DH + k] * bf2f(wo[(size_t)k * DM + c]);
  for (int k = ks; k < FF; k += 4) bb += h[b * FF + k] * bf2f(wd[(size_t)k * DM + c]);
  __shared__ float r[4][64];
  float gg = g[b];
  r[ks][threadIdx.x & 63] = gg * a + (1.f - gg) * bb;
  __syncthreads();
  if (ks == 0) out[b * DM + c] = f2bf(r[0][threadIdx.x] + r[1][threadIdx.x] + r[2][threadIdx.x] + r[3][threadIdx.x]);
}

extern "C" void kernel_launch(void* const* d_in, const int* in_sizes, int n_in, void* d_out, int out_size, void* d_ws,
                              size_t ws_size, hipStream_t stream) {
  const u16* x = (const u16*)d_in[0];
  const u16* kc = (const u16*)d_in[1];
  const u16* vc = (const u16*)d_in[2];
  const u16* wq = (const u16*)d_in[3];
  const u16* wo = (const u16*)d_in[4];
  const u16* wg = (const u16*)d_in[6];
  const u16* wu = (const u16*)d_in[7];
  const u16* wd = (const u16*)d_in[8];
  const u16* gw = (const u16*)d_in[9];
  float* ws = (float*)d_ws;
  float* q = ws;
  float* h = q + 4 * 4096;
  float* g = h + 4 * FF;
  float* pm = g + 64;
  float* pl = pm + 1024;
  float* po = pl + 1024;
  float* o = po + 128 * 8 * 128;
  int n1 = HQ * DH + FF + 4;
  k_proj<<<(n1 + 63) / 64, 64, 0, stream>>>(x, wq, wg, wu, gw, q, h, g);
  k_attn<<<dim3(NSPLIT, HKV, NB), 256, 0, stream>>>(kc, vc, q, pm, pl, po);
  k_comb<<<NB * HQ, DH, 0, stream>>>(pm, pl, po, o);
  k_out<<<dim3(DM / 64, NB), 256, 0, stream>>>(o, h, g, wo, wd, (u16*)d_out);
}
```

```cpp
#include <hip/hip_runtime.h>
#include <cstdint>

#define NBUF 5
#define STAMPS 0
#define W_AUX 2
#define QPRE 4
#define PREK 4
#define PREV 4
#define EXP_NOTILE 0
#define EXP_NOTW 0
#define EXP_NOX 0
#define EXP_NOQ 0
#define X_AUX 0
#define SLP1 2
#define SLP2 2
#define SPEC 2

typedef unsigned v4u __attribute__((ext_vector_type(4)));
typedef unsigned v2u __attribute__((ext_vector_type(2)));
typedef float f32x4 __attribute__((ext_vector_type(4)));
typedef float f32x2 __attribute__((ext_vector_type(2)));
typedef short s16x4 __attribute__((ext_vector_type(4)));
typedef __bf16 bf16x8 __attribute__((ext_vector_type(8)));
typedef __bf16 bf16x2 __attribute__((ext_vector_type(2)));
typedef __amdgpu_buffer_rsrc_t rsrc_t;
#define LAS __attribute__((address_space(3)))

namespace fk {
constexpr int AUX_NT = 2, AUX_SC1 = 16;
constexpr unsigned OOB = 0x40000000u;
constexpr unsigned WS_QG = 4096;
constexpr unsigned WS_GU = WS_QG + 65536;
constexpr unsigned WS_ST = WS_GU + 57344;
constexpr unsigned WS_PG = WS_ST + 8192;
constexpr unsigned WS_FG = WS_PG + 524288;
constexpr unsigned WS_END = WS_FG + 524288;
constexpr unsigned WS_STAMP = 8u << 20;
constexpr unsigned SPIN_CAP = 1u << 17;

constexpr int L_RED = 0;
constexpr int L_Q = 4096;
constexpr int L_H = L_Q + 1024;
constexpr int L_GATE = L_H + 256;
constexpr int L_FLAG = L_GATE + 16;
constexpr int L_O = L_FLAG + 16;
constexpr int L_ML = L_O + 512;
constexpr int L_MRG = L_ML + 256;
constexpr int L_END = L_MRG + 16384;

__device__ __forceinline__ rsrc_t mk(const void* p, unsigned bytes) {
  return __builtin_amdgcn_make_buffer_rsrc((void*)p, 0, (int)bytes, 0x00020000);
}
__device__ __forceinline__ float dot2(unsigned a, unsigned b, float c) {
  return __builtin_amdgcn_fdot2_f32_bf16(__builtin_bit_cast(bf16x2, a), __builtin_bit_cast(bf16x2, b), c, false);
}
__device__ __forceinline__ unsigned pk_bf16(float lo, float hi) {
  f32x2 v = {lo, hi};
  return __builtin_bit_cast(unsigned, __builtin_convertvector(v, bf16x2));
}
template <int CTRL> __device__ __forceinline__ float dpp(float x) {
  return __builtin_bit_cast(float, __builtin_amdgcn_mov_dpp(__builtin_bit_cast(int, x), CTRL, 0xf, 0xf, true));
}
__device__ __forceinline__ float swap32_add(float a, float b) {
  auto s = __builtin_amdgcn_permlane32_swap(__float_as_uint(a), __float_as_uint(b), false, false);
  return __uint_as_float(s[0]) + __uint_as_float(s[1]);
}
__device__ __forceinline__ float swap16_add(float a, float b) {
  auto s = __builtin_amdgcn_permlane16_swap(__float_as_uint(a), __float_as_uint(b), false, false);
  return __uint_as_float(s[0]) + __uint_as_float(s[1]);
}
__device__ __forceinline__ float xrow_max(float x) {
  auto s = __builtin_amdgcn_permlane16_swap(__float_as_uint(x), __float_as_uint(x), false, false);
  x = fmaxf(__uint_as_float(s[0]), __uint_as_float(s[1]));
  auto t = __builtin_amdgcn_permlane32_swap(__float_as_uint(x), __float_as_uint(x), false, false);
  return fmaxf(__uint_as_float(t[0]), __uint_as_float(t[1]));
}
__device__ __forceinline__ float xrow_sum(float x) {
  x = swap16_add(x, x);
  return swap32_add(x, x);
}
constexpr int ROW_SHL = 0x100, ROW_SHR = 0x110, ROW_ROR = 0x120;
__device__ __forceinline__ f32x2 treduce16(float (&v)[32], int lane) {
#pragma unroll
  for (int i = 0; i < 16; ++i) v[i] = swap32_add(v[i], v[i + 16]);
#pragma unroll
  for (int i = 0; i < 8; ++i) v[i] = swap16_add(v[i], v[i + 8]);
#pragma unroll
  for (int i = 0; i < 4; ++i) { float t1 = v[i] + dpp<ROW_ROR + 8>(v[i]), t2 = v[i + 4] + dpp<ROW_ROR + 8>(v[i + 4]); v[i] = (lane & 8) ? t2 : t1; }
#pragma unroll
  for (int i = 0; i < 2; ++i) { float t1 = v[i] + dpp<ROW_SHL + 4>(v[i]), t2 = v[i + 2] + dpp<ROW_SHR + 4>(v[i + 2]); v[i] = (lane & 4) ? t2 : t1; }
  return f32x2{v[0], v[1]};
}
template <bool FIRST>
__device__ __forceinline__ void rowpair(float (&v)[32], const v4u& w0, const v4u& w1, unsigned x0, unsigned x1, unsigned x2, unsigned x3) {
  const unsigned xp[4] = {x0, x1, x2, x3};
#pragma unroll
  for (int d = 0; d < 4; ++d) {
    const unsigned lo = __builtin_amdgcn_perm(w1[d], w0[d], 0x05040100u), hi = __builtin_amdgcn_perm(w1[d], w0[d], 0x07060302u);
#pragma unroll
    for (int b = 0; b < 4; ++b) {
      v[b * 8 + 2 * d] = dot2(lo, xp[b], FIRST ? 0.f : v[b * 8 + 2 * d]);
      v[b * 8 + 2 * d + 1] = dot2(hi, xp[b], FIRST ? 0.f : v[b * 8 + 2 * d + 1]);
    }
  }
}
__device__ __forceinline__ f32x4 mfma32(v4u a, v4u b, f32x4 c) {
  return __builtin_amdgcn_mfma_f32_16x16x32_bf16(__builtin_bit_cast(bf16x8, a), __builtin_bit_cast(bf16x8, b), c, 0, 0, 0);
}
__device__ __forceinline__ f32x4 mfma16(v2u a, v2u b, f32x4 c) {
  return __builtin_amdgcn_mfma_f32_16x16x16bf16_1k(__builtin_bit_cast(s16x4, a), __builtin_bit_cast(s16x4, b), c, 0, 0, 0);
}
__device__ __forceinline__ float sel4(const f32x4& d, int g) { return g == 0 ? d[0] : g == 1 ? d[1] : g == 2 ? d[2] : d[3]; }
__device__ __forceinline__ unsigned lds_inc(LAS unsigned char* p, int lane) {
  unsigned old = 0;
  if (lane == 0) old = __hip_atomic_fetch_add((LAS unsigned*)p, 1u, __ATOMIC_RELAXED, __HIP_MEMORY_SCOPE_WORKGROUP);
  return __builtin_amdgcn_readfirstlane(old);
}
__device__ __forceinline__ unsigned emb(float v, unsigned t8) { return ((__float_as_uint(v) + 0x80u) & 0xffffff00u) | t8; }
__device__ __forceinline__ bool emb_ok(const v4u& a, unsigned t8) { return (((a.x ^ t8) | (a.y ^ t8) | (a.z ^ t8) | (a.w ^ t8)) & 0xffu) == 0u; }
__device__ __forceinline__ float unemb(unsigned u) { return __uint_as_float(u & 0xffffff00u); }
__device__ __forceinline__ void tr8x4(const v2u (&r)[8], v4u (&f)[4]) {
#pragma unroll
  for (int i = 0; i < 4; ++i) {
    f[0][i] = __builtin_amdgcn_perm(r[2 * i + 1].x, r[2 * i].x, 0x05040100u); f[1][i] = __builtin_amdgcn_perm(r[2 * i + 1].x, r[2 * i].x, 0x07060302u);
    f[2][i] = __builtin_amdgcn_perm(r[2 * i + 1].y, r[2 * i].y, 0x05040100u); f[3][i] = __builtin_amdgcn_perm(r[2 * i + 1].y, r[2 * i].y, 0x07060302u);
  }
}
#define SB() __builtin_amdgcn_sched_barrier(0)
#define CB() asm volatile("" ::: "memory")
#define LGKM0() asm volatile("s_waitcnt lgkmcnt(0)" ::: "memory")
#define BAR() do { LGKM0(); __builtin_amdgcn_s_barrier(); CB(); } while (0)
#define STAMP(i) do { } while (0)

__global__ void __launch_bounds__(512, 2)
fused_kernel(const void* __restrict__ px, const void* __restrict__ pk, const void* __restrict__ pv, const void* __restrict__ pwq,
             const void* __restrict__ pwo, const void* __restrict__ pwg, const void* __restrict__ pwu, const void* __restrict__ pwd,
             const void* __restrict__ pgw, void* __restrict__ pout, void* pws) {
  __shared__ __attribute__((aligned(16))) unsigned char smem[L_END];
  LAS unsigned char* L = (LAS unsigned char*)smem;
  STAMP(0);
  asm volatile("" :: "s"(px), "s"(pk), "s"(pv), "s"(pwq), "s"(pwo), "s"(pwg), "s"(pwu), "s"(pwd), "s"(pgw), "s"(pout), "s"(pws));
  const int tid = threadIdx.x, lane = tid & 63, w = __builtin_amdgcn_readfirstlane(tid >> 6);
  const int g = lane >> 4, c = lane & 15;
  const int blk = blockIdx.x, kvh = blk & 7, r = blk >> 3;
  const int b_att = r >> 3, s_att = r & 7;
  const int rb = r >> 2, cq = r & 3;
  const int pidx = kvh * 8 + rb;

  const size_t kvoff = ((size_t)(b_att * 8 + kvh) * 8192 + s_att * 1024) * 256;
  const rsrc_t rk = mk((const char*)pk + kvoff, 262144), rv = mk((const char*)pv + kvoff, 262144);
  const rsrc_t rws = mk(pws, WS_END);
  const rsrc_t rx = mk(px, 4096), rgw = mk(pgw, 1024);
  const rsrc_t rwo = mk(pwo, 4096 * 512 * 2), rwd = mk(pwd, 1792 * 512 * 2);

  __builtin_amdgcn_s_setprio(3);
  const int rr4 = lane >> 2, ch = lane & 3;
  const bool isq = r < 16;
  const int um = (r - 16) >= 7 ? 1 : 0;
  const int ucb = kvh * 7 + (r - 16) - 7 * um;
  v4u wt[4]; v2u xq[4];
  {
    const int row = w * 64 + 4 * rr4;
    const unsigned rstr = isq ? 8192u : 3584u;
    const rsrc_t ru = mk(isq ? pwq : (um ? pwu : pwg), isq ? 512 * 4096 * 2 : 512 * 1792 * 2);
    const unsigned uo = (r < 30 && !EXP_NOTILE) ? (unsigned)row * rstr + (unsigned)((isq ? kvh * 16 + r : ucb) * 32 + ch * 8) * 2u : OOB;
#pragma unroll
    for (int i = 0; i < 4; ++i) wt[i] = __builtin_amdgcn_raw_buffer_load_b128(ru, uo + i * rstr, 0, W_AUX);
#pragma unroll
    for (int b = 0; b < 4; ++b) xq[b] = __builtin_amdgcn_raw_buffer_load_b64(rx, EXP_NOX ? OOB : row * 2 + b * 1024, 0, X_AUX);
  }
  SB();
  if (tid == 0) { *(volatile LAS v4u*)(L + L_FLAG) = v4u{0u, 0u, 0u, 0u}; }
  BAR();
  SB();
  const unsigned ko = c * 256 + g * 16, vo = g * 1024 + c * 16;
  v4u kb[NBUF][4], vb[NBUF][4];
  auto issueK = [&](int j, int i0, int i1) {
    const unsigned so = (unsigned)(j * 8 + w) * 4096u;
#pragma unroll
    for (int i = i0; i < i1; ++i) kb[j % NBUF][i] = __builtin_amdgcn_raw_buffer_load_b128(rk, ko + i * 64, so, AUX_NT);
    SB();
  };
  auto issueV = [&](int j, int i0, int i1) {
    const unsigned so = (unsigned)(j * 8 + w) * 4096u;
#pragma unroll
    for (int i = i0; i < i1; ++i) vb[j % NBUF][i] = __builtin_amdgcn_raw_buffer_load_b128(rv, vo + i * 256, so, AUX_NT);
    SB();
  };
  auto issue = [&](int j) { issueK(j, 0, 4); issueV(j, 0, 4); STAMP(11 + j); };
  const unsigned E = *(const __attribute__((address_space(4))) unsigned*)pws;
  SB();
  issueV(0, 0, PREV);
  issueK(0, 0, PREK);
  const unsigned tag = E * 0x9E3779B5u + 0x7F4A7C15u;
  const unsigned t8 = tag & 0xffu;
  LAS f32x2* red = (LAS f32x2*)(L + L_RED);
  {
    float v[32];
    rowpair<true>(v, wt[0], wt[1], xq[0].x, xq[1].x, xq[2].x, xq[3].x);
    rowpair<false>(v, wt[2], wt[3], xq[0].y, xq[1].y, xq[2].y, xq[3].y);
    red[w * 64 + lane] = treduce16(v, lane);
    LGKM0();
    const unsigned old = lds_inc(L + L_FLAG + 4, lane);
    CB();
    const f32x2 s = ((red[lane] + red[64 + lane]) + (red[128 + lane] + red[192 + lane])) + ((red[256 + lane] + red[320 + lane]) + (red[384 + lane] + red[448 + lane]));
    const int pr = ch * 4 + (rr4 & 3);
    const unsigned po = isq ? WS_QG + (unsigned)(((kvh * 4 + g) * 256 + r * 16 + pr) * 8) : WS_GU + (unsigned)(((um * 4 + g) * 896 + ucb * 16 + pr) * 8);
    __builtin_amdgcn_raw_buffer_store_b64(v2u{pk_bf16(s.x, s.y), tag}, rws, (old == 7 && r < 30) ? po : OOB, 0, AUX_SC1);
    SB();
    __builtin_amdgcn_s_setprio(0);
    SB();
  }
  STAMP(1);
  issueV(0, PREV, 4);
  issueK(0, PREK, 4);
  STAMP(11);
#pragma unroll
  for (int j = 1; j < QPRE; ++j) issue(j);
  const unsigned qgo = (w < 2 && !EXP_NOQ) ? WS_QG + (unsigned)((kvh * 4 + b_att) * 2048 + w * 1024 + lane * 16) : OOB;
  CB();
  v4u qa = __builtin_amdgcn_raw_buffer_load_b128(rws, qgo, 0, AUX_SC1);
  SB();
  issue(QPRE);
  if (w < 2) {
    if (!EXP_NOQ && __builtin_amdgcn_ballot_w64(qa.y == tag && qa.w == tag) != ~0ull) {
      STAMP(10);
      for (unsigned spin = 0; spin < SPIN_CAP; ++spin) {
        CB();
        qa = __builtin_amdgcn_raw_buffer_load_b128(rws, qgo, 0, AUX_SC1);
        if (__builtin_amdgcn_ballot_w64(qa.y == tag && qa.w == tag) == ~0ull) break;
      }
    }
    *(LAS v2u*)(L + L_Q + (w * 128 + lane * 2) * 4) = v2u{qa.x, qa.z};
    LGKM0();
    (void)lds_inc(L + L_FLAG, lane);
    STAMP(9);
  }
  SB();
#pragma unroll
  for (int j = QPRE + 1; j < NBUF; ++j) issue(j);
  for (unsigned spin = 0; spin < (SPIN_CAP << 4); ++spin) {
    if (*(volatile LAS unsigned*)(L + L_FLAG) >= 2u) break;
    __builtin_amdgcn_s_sleep(1);
  }
  CB();
  STAMP(2);
  v4u qf[4];
#pragma unroll
  for (int i = 0; i < 4; ++i) qf[i] = *(LAS v4u*)(L + L_Q + (((c & 3) * 64 + 16 * i + 4 * g) * 4));

  constexpr float C2 = 0.08838834764831845f * 1.4426950408889634f;
  f32x4 acc[8];
#pragma unroll
  for (int e = 0; e < 8; ++e) acc[e] = f32x4{0.f, 0.f, 0.f, 0.f};
  float m = -INFINITY, l = 0.f;
#pragma unroll
  for (int j = 0; j < 8; ++j) {
    const int bf = j % NBUF;
    f32x4 sc = {0.f, 0.f, 0.f, 0.f};
#pragma unroll
    for (int i = 0; i < 4; ++i) sc = mfma32(kb[bf][i], qf[i], sc);
    const float t0 = sc[0] * C2, t1 = sc[1] * C2, t2 = sc[2] * C2, t3 = sc[3] * C2;
    const float cm = xrow_max(fmaxf(fmaxf(t0, t1), fmaxf(t2, t3)));
    const float mn = fmaxf(m, cm);
    const float alpha = __builtin_amdgcn_exp2f(m - mn);
    const float p0 = __builtin_amdgcn_exp2f(t0 - mn), p1 = __builtin_amdgcn_exp2f(t1 - mn), p2 = __builtin_amdgcn_exp2f(t2 - mn), p3 = __builtin_amdgcn_exp2f(t3 - mn);
    l = l * alpha + ((p0 + p1) + (p2 + p3));
    m = mn;
    const v2u pa = {pk_bf16(p0, p1), pk_bf16(p2, p3)};
    if (j > 0) {
      const float a0 = dpp<0x00>(alpha), a1 = dpp<0x55>(alpha), a2 = dpp<0xAA>(alpha), a3 = dpp<0xFF>(alpha);
#pragma unroll
      for (int e = 0; e < 8; ++e) { acc[e][0] *= a0; acc[e][1] *= a1; acc[e][2] *= a2; acc[e][3] *= a3; }
    }
#pragma unroll
    for (int d = 0; d < 4; ++d) {
      const v2u lo = {__builtin_amdgcn_perm(vb[bf][1][d], vb[bf][0][d], 0x05040100u), __builtin_amdgcn_perm(vb[bf][3][d], vb[bf][2][d], 0x05040100u)};
      const v2u hi = {__builtin_amdgcn_perm(vb[bf][1][d], vb[bf][0][d], 0x07060302u), __builtin_amdgcn_perm(vb[bf][3][d], vb[bf][2][d], 0x07060302u)};
      acc[2 * d] = mfma16(pa, lo, acc[2 * d]);
      acc[2 * d + 1] = mfma16(pa, hi, acc[2 * d + 1]);
    }
    SB();
    if (j + NBUF < 8) issue(j + NBUF);
  }
  STAMP(3);
  l = xrow_sum(l);
  if (g == 0) {
    LAS float* mo = (LAS float*)(L + L_MRG) + w * 512 + 8 * c;
#pragma unroll
    for (int h = 0; h < 4; ++h) {
      *(LAS f32x4*)(mo + h * 128) = f32x4{acc[0][h], acc[1][h], acc[2][h], acc[3][h]};
      *(LAS f32x4*)(mo + h * 128 + 4) = f32x4{acc[4][h], acc[5][h], acc[6][h], acc[7][h]};
    }
    if (c < 4) *(LAS f32x2*)(L + L_ML + (c * 8 + w) * 8) = f32x2{m, l};
  }
  v4u bo[2][4], bd[4];
  if (w < 2) {
    const unsigned colb = (unsigned)(cq * 128 + w * 64 + 4 * c) * 2u;
    v2u r0[8], r1[8], r2[8];
    const unsigned wob = EXP_NOTW ? OOB : (unsigned)(kvh * 512 + rb * 64 + 8 * g) * 1024u + colb;
#pragma unroll
    for (int i = 0; i < 8; ++i) r0[i] = __builtin_amdgcn_raw_buffer_load_b64(rwo, wob + i * 1024, 0, W_AUX);
#pragma unroll
    for (int i = 0; i < 8; ++i) r1[i] = __builtin_amdgcn_raw_buffer_load_b64(rwo, wob + 32768 + i * 1024, 0, W_AUX);
    const unsigned wdb = (unsigned)(pidx * 28 + 8 * g) * 1024u + colb;
#pragma unroll
    for (int i = 0; i < 8; ++i) r2[i] = __builtin_amdgcn_raw_buffer_load_b64(rwd, (8 * g + i < 28 && !EXP_NOTW) ? wdb + i * 1024 : OOB, 0, W_AUX);
    SB();
    tr8x4(r0, bo[0]); tr8x4(r1, bo[1]); tr8x4(r2, bd);
  } else if (w == 2) {
    const unsigned go = c < 14 ? WS_GU + (unsigned)((g * 896 + pidx * 14 + c) * 8) : OOB;
    v4u xv[4];
#pragma unroll
    for (int b = 0; b < 4; ++b) xv[b] = __builtin_amdgcn_raw_buffer_load_b128(rx, lane * 16 + b * 1024, 0, 0);
    const v4u gv = __builtin_amdgcn_raw_buffer_load_b128(rgw, lane * 16, 0, 0);
    v2u ga = {0, 0}, ua = {0, 0};
    for (unsigned spin = 0; spin < SPIN_CAP; ++spin) {
      CB();
      ga = __builtin_amdgcn_raw_buffer_load_b64(rws, go, 0, AUX_SC1);
      ua = __builtin_amdgcn_raw_buffer_load_b64(rws, go + 28672, 0, AUX_SC1);
      if (__builtin_amdgcn_ballot_w64(c >= 14 || (ga.y == tag && ua.y == tag)) == ~0ull) break;
      __builtin_amdgcn_s_sleep(2);
    }
    const float g0 = __uint_as_float(ga.x << 16), g1 = __uint_as_float(ga.x & 0xffff0000u), u0 = __uint_as_float(ua.x << 16), u1 = __uint_as_float(ua.x & 0xffff0000u);
    const float h0 = u0 / (1.f + __expf(-g0)), h1 = u1 / (1.f + __expf(-g1));
    *(LAS unsigned*)(L + L_H + (g * 16 + c) * 4) = c < 14 ? pk_bf16(h0, h1) : 0u;
    float a[4];
#pragma unroll
    for (int b = 0; b < 4; ++b) { a[b] = dot2(xv[b].x, gv.x, 0.f); a[b] = dot2(xv[b].y, gv.y, a[b]); a[b] = dot2(xv[b].z, gv.z, a[b]); a[b] = dot2(xv[b].w, gv.w, a[b]); }
    a[0] = swap32_add(a[0], a[2]); a[1] = swap32_add(a[1], a[3]);
    a[0] = swap16_add(a[0], a[1]);
    a[0] += dpp<ROW_ROR + 8>(a[0]); a[0] += dpp<ROW_ROR + 4>(a[0]); a[0] += dpp<ROW_ROR + 2>(a[0]); a[0] += dpp<ROW_ROR + 1>(a[0]);
    if (c == 0) *(LAS float*)(L + L_GATE + g * 4) = 1.f / (1.f + __expf(-a[0]));
  }
  BAR();
  {
    const int hd = w >> 1, dim = tid & 127;
    f32x4 ml[4]; float ov[8];
#pragma unroll
    for (int i = 0; i < 4; ++i) ml[i] = *(LAS f32x4*)(L + L_ML + hd * 64 + i * 16);
#pragma unroll
    for (int i = 0; i < 8; ++i) ov[i] = ((LAS float*)(L + L_MRG))[i * 512 + hd * 128 + dim];
    const float M = fmaxf(fmaxf(fmaxf(ml[0][0], ml[0][2]), fmaxf(ml[1][0], ml[1][2])), fmaxf(fmaxf(ml[2][0], ml[2][2]), fmaxf(ml[3][0], ml[3][2])));
    float Ls = 0.f, o = 0.f;
#pragma unroll
    for (int i = 0; i < 4; ++i) {
      const float w0 = __builtin_amdgcn_exp2f(ml[i][0] - M), w1 = __builtin_amdgcn_exp2f(ml[i][2] - M);
      Ls += w0 * ml[i][1] + w1 * ml[i][3]; o += w0 * ov[2 * i] + w1 * ov[2 * i + 1];
    }
    o *= __builtin_amdgcn_rcpf(Ls);
    const unsigned lseu = __float_as_uint(M + __builtin_amdgcn_logf(Ls));
    const unsigned nib = (lseu >> ((dim & 7) * 4)) & 15u;
    __builtin_amdgcn_raw_buffer_store_b32(emb(o, (t8 & 0x0fu) | (nib << 4)), rws, WS_PG + (unsigned)(((kvh * 32 + r) * 512 + tid) * 4), 0, AUX_SC1);
    SB();
  }
  STAMP(4);
  const f32x4 zero = {0.f, 0.f, 0.f, 0.f};
  f32x4 db[4];
  float gate = 0.f;
  if (w < 2) {
    const v4u hf = *(LAS v4u*)(L + L_H + ((c & 3) * 32 + 8 * g) * 2);
    gate = *(LAS float*)(L + L_GATE + g * 4);
#pragma unroll
    for (int e = 0; e < 4; ++e) db[e] = mfma32(hf, bd[e], zero);
  }
  {
    const int bb = w >> 1, sp = lane >> 3;
    const unsigned po = WS_PG + (unsigned)(((kvh * 32 + bb * 8 + sp) * 512 + rb * 64 + (w & 1) * 32 + (lane & 7) * 4) * 4);
    v4u pl = {0, 0, 0, 0};
    bool pok = false;
    for (unsigned spin = 0; spin < SPIN_CAP; ++spin) {
      CB();
      const v4u a = __builtin_amdgcn_raw_buffer_load_b128(rws, pok ? OOB : po, 0, AUX_SC1);
      const bool mine = (((a.x ^ t8) | (a.y ^ t8) | (a.z ^ t8) | (a.w ^ t8)) & 0x0fu) == 0u;
      const unsigned long long bm = __builtin_amdgcn_ballot_w64(mine);
      if (!pok && ((bm >> (lane & 56)) & 0xffull) == 0xffull) { pok = true; pl = a; }
      if (__builtin_amdgcn_ballot_w64(!pok) == 0ull) break;
      __builtin_amdgcn_s_sleep(SLP1);
    }
    unsigned half16 = ((pl.x >> 4) & 15u) | (pl.y & 0xf0u) | ((pl.z & 0xf0u) << 4) | ((pl.w & 0xf0u) << 8);
    const unsigned other = __builtin_amdgcn_mov_dpp(half16, 0xB1, 0xf, 0xf, true);
    const float lse = __uint_as_float((lane & 1) ? (other | (half16 << 16)) : (half16 | (other << 16)));
    float M = fmaxf(lse, dpp<ROW_ROR + 8>(lse)); M = xrow_max(M);
    const float wt_ = __builtin_amdgcn_exp2f(lse - M);
    float v0 = wt_ * unemb(pl.x), v1 = wt_ * unemb(pl.y), v2 = wt_ * unemb(pl.z), v3 = wt_ * unemb(pl.w), W = wt_;
    W += dpp<ROW_ROR + 8>(W); W = xrow_sum(W);
    v0 = swap32_add(v0, v2); v1 = swap32_add(v1, v3);
    v0 = swap16_add(v0, v0); v1 = swap16_add(v1, v1);
    v0 += dpp<ROW_ROR + 8>(v0); v1 += dpp<ROW_ROR + 8>(v1);
    const float inv = __builtin_amdgcn_rcpf(W);
    if ((lane & 24) == 0) *(LAS unsigned*)(L + L_O + (bb * 64 + (w & 1) * 32 + (lane & 7) * 4 + (lane >> 5) * 2) * 2) = pk_bf16(v0 * inv, v1 * inv);
  }
  BAR();
  STAMP(5);
  if (w < 2) {
    const v4u of0 = *(LAS v4u*)(L + L_O + ((c & 3) * 64 + 8 * g) * 2), of1 = *(LAS v4u*)(L + L_O + ((c & 3) * 64 + 32 + 8 * g) * 2);
    v4u res;
#pragma unroll
    for (int e = 0; e < 4; ++e) {
      f32x4 da = mfma32(of0, bo[0][e], zero);
      da = mfma32(of1, bo[1][e], da);
      res[e] = emb(gate * sel4(da, g) + (1.f - gate) * sel4(db[e], g), t8);
    }
    const int R = g * 16 + cq * 4 + w * 2 + (c >> 3);
    __builtin_amdgcn_raw_buffer_store_b128(res, rws, WS_FG + (unsigned)(((R * 64 + pidx) * 32 + (c & 7) * 4) * 4), 0, AUX_SC1);
    SB();
  }
  STAMP(6);
  if ((r & 3) == 0) {
    const int R = (r >> 2) * 8 + kvh;
    const unsigned fo = WS_FG + (unsigned)(R * 8192 + w * 1024 + lane * 16);
    v4u pl = {0, 0, 0, 0};
    bool ok = false;
    for (unsigned spin = 0; spin < SPIN_CAP; ++spin) {
      CB();
      const v4u a = __builtin_amdgcn_raw_buffer_load_b128(rws, ok ? OOB : fo, 0, AUX_SC1);
      if (!ok && emb_ok(a, t8)) { ok = true; pl = a; }
      if (__builtin_amdgcn_ballot_w64(!ok) == 0ull) break;
      __builtin_amdgcn_s_sleep(SLP2);
    }
    float v0 = unemb(pl.x), v1 = unemb(pl.y), v2 = unemb(pl.z), v3 = unemb(pl.w);
    v0 = swap32_add(v0, v2); v1 = swap32_add(v1, v3);
    v0 = swap16_add(v0, v0); v1 = swap16_add(v1, v1);
    v0 += dpp<ROW_ROR + 8>(v0); v1 += dpp<ROW_ROR + 8>(v1);
    if ((lane & 24) == 0) *(LAS f32x2*)(L + L_RED + (w * 32 + (lane & 7) * 4 + (lane >> 5) * 2) * 4) = f32x2{v0, v1};
    BAR();
    if (w == 0 && lane < 16) {
      const LAS f32x2* fr = (const LAS f32x2*)(L + L_RED) + lane;
      const f32x2 s = ((fr[0] + fr[16]) + (fr[32] + fr[48])) + ((fr[64] + fr[80]) + (fr[96] + fr[112]));
      ((unsigned*)pout)[R * 16 + lane] = pk_bf16(s.x, s.y);
      if (blk == 0 && lane == 0) __hip_atomic_store((unsigned*)pws, tag, __ATOMIC_RELAXED, __HIP_MEMORY_SCOPE_AGENT);
    }
  }
  STAMP(7);
}
}

extern "C" void kernel_launch(void* const* d_in, const int* in_sizes, int n_in, void* d_out, int out_size, void* d_ws,
                              size_t ws_size, hipStream_t stream) {
  fk::fused_kernel<<<256, 512, 0, stream>>>(d_in[0], d_in[1], d_in[2], d_in[3], d_in[4], d_in[6], d_in[7], d_in[8], d_in[9], d_out, d_ws);
}
```

```cpp
#define K_AUX 2
#define V_AUX 2
#define W_AUX 0
#include <hip/hip_runtime.h>
#include <cstdint>

#define NBUF 5
#define STAMPS 0
#define QPRE 4
#define PREK 4
#define PREV 4
#define EXP_NOTILE 0
#define EXP_NOTW 0
#define EXP_NOX 0
#define EXP_NOQ 0
#define X_AUX 0
#define SLP1 2
#define SLP2 2
#define SPEC 2

typedef unsigned v4u __attribute__((ext_vector_type(4)));
typedef unsigned v2u __attribute__((ext_vector_type(2)));
typedef float f32x4 __attribute__((ext_vector_type(4)));
typedef float f32x2 __attribute__((ext_vector_type(2)));
typedef short s16x4 __attribute__((ext_vector_type(4)));
typedef __bf16 bf16x8 __attribute__((ext_vector_type(8)));
typedef __bf16 bf16x2 __attribute__((ext_vector_type(2)));
typedef __amdgpu_buffer_rsrc_t rsrc_t;
#define LAS __attribute__((address_space(3)))

namespace fk {
constexpr int AUX_NT = 2, AUX_SC1 = 16;
constexpr unsigned OOB = 0x40000000u;
constexpr unsigned WS_QG = 4096;
constexpr unsigned WS_GU = WS_QG + 65536;
constexpr unsigned WS_ST = WS_GU + 57344;
constexpr unsigned WS_PG = WS_ST + 8192;
constexpr unsigned WS_FG = WS_PG + 524288;
constexpr unsigned WS_END = WS_FG + 524288;
constexpr unsigned WS_STAMP = 8u << 20;
constexpr unsigned SPIN_CAP = 1u << 17;

constexpr int L_RED = 0;
constexpr int L_Q = 4096;
constexpr int L_H = L_Q + 1024;
constexpr int L_GATE = L_H + 256;
constexpr int L_FLAG = L_GATE + 16;
constexpr int L_O = L_FLAG + 16;
constexpr int L_ML = L_O + 512;
constexpr int L_MRG = L_ML + 256;
constexpr int L_END = L_MRG + 16384;

__device__ __forceinline__ rsrc_t mk(const void* p, unsigned bytes) {
  return __builtin_amdgcn_make_buffer_rsrc((void*)p, 0, (int)bytes, 0x00020000);
}
__device__ __forceinline__ float dot2(unsigned a, unsigned b, float c) {
  return __builtin_amdgcn_fdot2_f32_bf16(__builtin_bit_cast(bf16x2, a), __builtin_bit_cast(bf16x2, b), c, false);
}
__device__ __forceinline__ unsigned pk_bf16(float lo, float hi) {
  f32x2 v = {lo, hi};
  return __builtin_bit_cast(unsigned, __builtin_convertvector(v, bf16x2));
}
template <int CTRL> __device__ __forceinline__ float dpp(float x) {
  return __builtin_bit_cast(float, __builtin_amdgcn_mov_dpp(__builtin_bit_cast(int, x), CTRL, 0xf, 0xf, true));
}
__device__ __forceinline__ float swap32_add(float a, float b) {
  auto s = __builtin_amdgcn_permlane32_swap(__float_as_uint(a), __float_as_uint(b), false, false);
  return __uint_as_float(s[0]) + __uint_as_float(s[1]);
}
__device__ __forceinline__ float swap16_add(float a, float b) {
  auto s = __builtin_amdgcn_permlane16_swap(__float_as_uint(a), __float_as_uint(b), false, false);
  return __uint_as_float(s[0]) + __uint_as_float(s[1]);
}
__device__ __forceinline__ float xrow_max(float x) {
  auto s = __builtin_amdgcn_permlane16_swap(__float_as_uint(x), __float_as_uint(x), false, false);
  x = fmaxf(__uint_as_float(s[0]), __uint_as_float(s[1]));
  auto t = __builtin_amdgcn_permlane32_swap(__float_as_uint(x), __float_as_uint(x), false, false);
  return fmaxf(__uint_as_float(t[0]), __uint_as_float(t[1]));
}
__device__ __forceinline__ float xrow_sum(float x) {
  x = swap16_add(x, x);
  return swap32_add(x, x);
}
constexpr int ROW_SHL = 0x100, ROW_SHR = 0x110, ROW_ROR = 0x120;
__device__ __forceinline__ f32x2 treduce16(float (&v)[32], int lane) {
#pragma unroll
  for (int i = 0; i < 16; ++i) v[i] = swap32_add(v[i], v[i + 16]);
#pragma unroll
  for (int i = 0; i < 8; ++i) v[i] = swap16_add(v[i], v[i + 8]);
#pragma unroll
  for (int i = 0; i < 4; ++i) { float t1 = v[i] + dpp<ROW_ROR + 8>(v[i]), t2 = v[i + 4] + dpp<ROW_ROR + 8>(v[i + 4]); v[i] = (lane & 8) ? t2 : t1; }
#pragma unroll
  for (int i = 0; i < 2; ++i) { float t1 = v[i] + dpp<ROW_SHL + 4>(v[i]), t2 = v[i + 2] + dpp<ROW_SHR + 4>(v[i + 2]); v[i] = (lane & 4) ? t2 : t1; }
  return f32x2{v[0], v[1]};
}
template <bool FIRST>
__device__ __forceinline__ void rowpair(float (&v)[32], const v4u& w0, const v4u& w1, unsigned x0, unsigned x1, unsigned x2, unsigned x3) {
  const unsigned xp[4] = {x0, x1, x2, x3};
#pragma unroll
  for (int d = 0; d < 4; ++d) {
    const unsigned lo = __builtin_amdgcn_perm(w1[d], w0[d], 0x05040100u), hi = __builtin_amdgcn_perm(w1[d], w0[d], 0x07060302u);
#pragma unroll
    for (int b = 0; b < 4; ++b) {
      v[b * 8 + 2 * d] = dot2(lo, xp[b], FIRST ? 0.f : v[b * 8 + 2 * d]);
      v[b * 8 + 2 * d + 1] = dot2(hi, xp[b], FIRST ? 0.f : v[b * 8 + 2 * d + 1]);
    }
  }
}
__device__ __forceinline__ f32x4 mfma32(v4u a, v4u b, f32x4 c) {
  return __builtin_amdgcn_mfma_f32_16x16x32_bf16(__builtin_bit_cast(bf16x8, a), __builtin_bit_cast(bf16x8, b), c, 0, 0, 0);
}
__device__ __forceinline__ f32x4 mfma16(v2u a, v2u b, f32x4 c) {
  return __builtin_amdgcn_mfma_f32_16x16x16bf16_1k(__builtin_bit_cast(s16x4, a), __builtin_bit_cast(s16x4, b), c, 0, 0, 0);
}
__device__ __forceinline__ float sel4(const f32x4& d, int g) { return g == 0 ? d[0] : g == 1 ? d[1] : g == 2 ? d[2] : d[3]; }
__device__ __forceinline__ unsigned lds_inc(LAS unsigned char* p, int lane) {
  unsigned old = 0;
  if (lane == 0) old = __hip_atomic_fetch_add((LAS unsigned*)p, 1u, __ATOMIC_RELAXED, __HIP_MEMORY_SCOPE_WORKGROUP);
  return __builtin_amdgcn_readfirstlane(old);
}
__device__ __forceinline__ unsigned emb(float v, unsigned t8) { return ((__float_as_uint(v) + 0x80u) & 0xffffff00u) | t8; }
__device__ __forceinline__ bool emb_ok(const v4u& a, unsigned t8) { return (((a.x ^ t8) | (a.y ^ t8) | (a.z ^ t8) | (a.w ^ t8)) & 0xffu) == 0u; }
__device__ __forceinline__ float unemb(unsigned u) { return __uint_as_float(u & 0xffffff00u); }
__device__ __forceinline__ void tr8x4(const v2u (&r)[8], v4u (&f)[4]) {
#pragma unroll
  for (int i = 0; i < 4; ++i) {
    f[0][i] = __builtin_amdgcn_perm(r[2 * i + 1].x, r[2 * i].x, 0x05040100u); f[1][i] = __builtin_amdgcn_perm(r[2 * i + 1].x, r[2 * i].x, 0x07060302u);
    f[2][i] = __builtin_amdgcn_perm(r[2 * i + 1].y, r[2 * i].y, 0x05040100u); f[3][i] = __builtin_amdgcn_perm(r[2 * i + 1].y, r[2 * i].y, 0x07060302u);
  }
}
#define SB() __builtin_amdgcn_sched_barrier(0)
#define CB() asm volatile("" ::: "memory")
#define LGKM0() asm volatile("s_waitcnt lgkmcnt(0)" ::: "memory")
#define BAR() do { LGKM0(); __builtin_amdgcn_s_barrier(); CB(); } while (0)
#define STAMP(i) do { } while (0)

__global__ void __launch_bounds__(512, 2)
fused_kernel(const void* __restrict__ px, const void* __restrict__ pk, const void* __restrict__ pv, const void* __restrict__ pwq,
             const void* __restrict__ pwo, const void* __restrict__ pwg, const void* __restrict__ pwu, const void* __restrict__ pwd,
             const void* __restrict__ pgw, void* __restrict__ pout, void* pws) {
  __shared__ __attribute__((aligned(16))) unsigned char smem[L_END];
  LAS unsigned char* L = (LAS unsigned char*)smem;
  STAMP(0);
  asm volatile("" :: "s"(px), "s"(pk), "s"(pv), "s"(pwq), "s"(pwo), "s"(pwg), "s"(pwu), "s"(pwd), "s"(pgw), "s"(pout), "s"(pws));
  const int tid = threadIdx.x, lane = tid & 63, w = __builtin_amdgcn_readfirstlane(tid >> 6);
  const int g = lane >> 4, c = lane & 15;
  const int blk = blockIdx.x, kvh = blk & 7, r = blk >> 3;
  const int b_att = r >> 3, s_att = r & 7;
  const int rb = r >> 2, cq = r & 3;
  const int pidx = kvh * 8 + rb;

  const size_t kvoff = ((size_t)(b_att * 8 + kvh) * 8192 + s_att * 1024) * 256;
  const rsrc_t rk = mk((const char*)pk + kvoff, 262144), rv = mk((const char*)pv + kvoff, 262144);
  const rsrc_t rws = mk(pws, WS_END);
  const rsrc_t rx = mk(px, 4096), rgw = mk(pgw, 1024);
  const rsrc_t rwo = mk(pwo, 4096 * 512 * 2), rwd = mk(pwd, 1792 * 512 * 2);

  __builtin_amdgcn_s_setprio(3);
  const int rr4 = lane >> 2, ch = lane & 3;
  const bool isq = r < 16;
  const int um = (r - 16) >= 7 ? 1 : 0;
  const int ucb = kvh * 7 + (r - 16) - 7 * um;
  v4u wt[4]; v2u xq[4];
  {
    const int row = w * 64 + 4 * rr4;
    const unsigned rstr = isq ? 8192u : 3584u;
    const rsrc_t ru = mk(isq ? pwq : (um ? pwu : pwg), isq ? 512 * 4096 * 2 : 512 * 1792 * 2);
    const unsigned uo = (r < 30 && !EXP_NOTILE) ? (unsigned)row * rstr + (unsigned)((isq ? kvh * 16 + r : ucb) * 32 + ch * 8) * 2u : OOB;
#pragma unroll
    for (int i = 0; i < 4; ++i) wt[i] = __builtin_amdgcn_raw_buffer_load_b128(ru, uo + i * rstr, 0, W_AUX);
#pragma unroll
    for (int b = 0; b < 4; ++b) xq[b] = __builtin_amdgcn_raw_buffer_load_b64(rx, EXP_NOX ? OOB : row * 2 + b * 1024, 0, X_AUX);
  }
  SB();
  if (tid == 0) { *(volatile LAS v4u*)(L + L_FLAG) = v4u{0u, 0u, 0u, 0u}; }
  BAR();
  SB();
  const unsigned ko = c * 256 + g * 16, vo = g * 1024 + c * 16;
  v4u kb[NBUF][4], vb[NBUF][4];
  auto issueK = [&](int j, int i0, int i1) {
    const unsigned so = (unsigned)(j * 8 + w) * 4096u;
#pragma unroll
    for (int i = i0; i < i1; ++i) kb[j % NBUF][i] = __builtin_amdgcn_raw_buffer_load_b128(rk, ko + i * 64, so, K_AUX);
    SB();
  };
  auto issueV = [&](int j, int i0, int i1) {
    const unsigned so = (unsigned)(j * 8 + w) * 4096u;
#pragma unroll
    for (int i = i0; i < i1; ++i) vb[j % NBUF][i] = __builtin_amdgcn_raw_buffer_load_b128(rv, vo + i * 256, so, V_AUX);
    SB();
  };
  auto issue = [&](int j) { issueK(j, 0, 4); issueV(j, 0, 4); STAMP(11 + j); };
  const unsigned E = *(const __attribute__((address_space(4))) unsigned*)pws;
  SB();
  issueV(0, 0, PREV);
  issueK(0, 0, PREK);
  const unsigned tag = E * 0x9E3779B5u + 0x7F4A7C15u;
  const unsigned t8 = tag & 0xffu;
  LAS f32x2* red = (LAS f32x2*)(L + L_RED);
  {
    float v[32];
    rowpair<true>(v, wt[0], wt[1], xq[0].x, xq[1].x, xq[2].x, xq[3].x);
    rowpair<false>(v, wt[2], wt[3], xq[0].y, xq[1].y, xq[2].y, xq[3].y);
    red[w * 64 + lane] = treduce16(v, lane);
    LGKM0();
    const unsigned old = lds_inc(L + L_FLAG + 4, lane);
    CB();
    const f32x2 s = ((red[lane] + red[64 + lane]) + (red[128 + lane] + red[192 + lane])) + ((red[256 + lane] + red[320 + lane]) + (red[384 + lane] + red[448 + lane]));
    const int pr = ch * 4 + (rr4 & 3);
    const unsigned po = isq ? WS_QG + (unsigned)(((kvh * 4 + g) * 256 + r * 16 + pr) * 8) : WS_GU + (unsigned)(((um * 4 + g) * 896 + ucb * 16 + pr) * 8);
    __builtin_amdgcn_raw_buffer_store_b64(v2u{pk_bf16(s.x, s.y), tag}, rws, (old == 7 && r < 30) ? po : OOB, 0, AUX_SC1);
    SB();
    __builtin_amdgcn_s_setprio(0);
    SB();
  }
  STAMP(1);
  issueV(0, PREV, 4);
  issueK(0, PREK, 4);
  STAMP(11);
#pragma unroll
  for (int j = 1; j < QPRE; ++j) issue(j);
  const unsigned qgo = (w < 2 && !EXP_NOQ) ? WS_QG + (unsigned)((kvh * 4 + b_att) * 2048 + w * 1024 + lane * 16) : OOB;
  CB();
  v4u qa = __builtin_amdgcn_raw_buffer_load_b128(rws, qgo, 0, AUX_SC1);
  SB();
  issue(QPRE);
  if (w < 2) {
    if (!EXP_NOQ && __builtin_amdgcn_ballot_w64(qa.y == tag && qa.w == tag) != ~0ull) {
      STAMP(10);
      for (unsigned spin = 0; spin < SPIN_CAP; ++spin) {
        CB();
        qa = __builtin_amdgcn_raw_buffer_load_b128(rws, qgo, 0, AUX_SC1);
        if (__builtin_amdgcn_ballot_w64(qa.y == tag && qa.w == tag) == ~0ull) break;
      }
    }
    *(LAS v2u*)(L + L_Q + (w * 128 + lane * 2) * 4) = v2u{qa.x, qa.z};
    LGKM0();
    (void)lds_inc(L + L_FLAG, lane);
    STAMP(9);
  }
  SB();
#pragma unroll
  for (int j = QPRE + 1; j < NBUF; ++j) issue(j);
  for (unsigned spin = 0; spin < (SPIN_CAP << 4); ++spin) {
    if (*(volatile LAS unsigned*)(L + L_FLAG) >= 2u) break;
    __builtin_amdgcn_s_sleep(1);
  }
  CB();
  STAMP(2);
  v4u qf[4];
#pragma unroll
  for (int i = 0; i < 4; ++i) qf[i] = *(LAS v4u*)(L + L_Q + (((c & 3) * 64 + 16 * i + 4 * g) * 4));

  constexpr float C2 = 0.08838834764831845f * 1.4426950408889634f;
  f32x4 acc[8];
#pragma unroll
  for (int e = 0; e < 8; ++e) acc[e] = f32x4{0.f, 0.f, 0.f, 0.f};
  float m = -INFINITY, l = 0.f;
#pragma unroll
  for (int j = 0; j < 8; ++j) {
    const int bf = j % NBUF;
    f32x4 sc = {0.f, 0.f, 0.f, 0.f};
#pragma unroll
    for (int i = 0; i < 4; ++i) sc = mfma32(kb[bf][i], qf[i], sc);
    const float t0 = sc[0] * C2, t1 = sc[1] * C2, t2 = sc[2] * C2, t3 = sc[3] * C2;
    const float cm = xrow_max(fmaxf(fmaxf(t0, t1), fmaxf(t2, t3)));
    const float mn = fmaxf(m, cm);
    const float alpha = __builtin_amdgcn_exp2f(m - mn);
    const float p0 = __builtin_amdgcn_exp2f(t0 - mn), p1 = __builtin_amdgcn_exp2f(t1 - mn), p2 = __builtin_amdgcn_exp2f(t2 - mn), p3 = __builtin_amdgcn_exp2f(t3 - mn);
    l = l * alpha + ((p0 + p1) + (p2 + p3));
    m = mn;
    const v2u pa = {pk_bf16(p0, p1), pk_bf16(p2, p3)};
    if (j > 0) {
      const float a0 = dpp<0x00>(alpha), a1 = dpp<0x55>(alpha), a2 = dpp<0xAA>(alpha), a3 = dpp<0xFF>(alpha);
#pragma unroll
      for (int e = 0; e < 8; ++e) { acc[e][0] *= a0; acc[e][1] *= a1; acc[e][2] *= a2; acc[e][3] *= a3; }
    }
#pragma unroll
    for (int d = 0; d < 4; ++d) {
      const v2u lo = {__builtin_amdgcn_perm(vb[bf][1][d], vb[bf][0][d], 0x05040100u), __builtin_amdgcn_perm(vb[bf][3][d], vb[bf][2][d], 0x05040100u)};
      const v2u hi = {__builtin_amdgcn_perm(vb[bf][1][d], vb[bf][0][d], 0x07060302u), __builtin_amdgcn_perm(vb[bf][3][d], vb[bf][2][d], 0x07060302u)};
      acc[2 * d] = mfma16(pa, lo, acc[2 * d]);
      acc[2 * d + 1] = mfma16(pa, hi, acc[2 * d + 1]);
    }
    SB();
    if (j + NBUF < 8) issue(j + NBUF);
  }
  STAMP(3);
  l = xrow_sum(l);
  if (g == 0) {
    LAS float* mo = (LAS float*)(L + L_MRG) + w * 512 + 8 * c;
#pragma unroll
    for (int h = 0; h < 4; ++h) {
      *(LAS f32x4*)(mo + h * 128) = f32x4{acc[0][h], acc[1][h], acc[2][h], acc[3][h]};
      *(LAS f32x4*)(mo + h * 128 + 4) = f32x4{acc[4][h], acc[5][h], acc[6][h], acc[7][h]};
    }
    if (c < 4) *(LAS f32x2*)(L + L_ML + (c * 8 + w) * 8) = f32x2{m, l};
  }
  v4u bo[2][4], bd[4];
  if (w < 2) {
    const unsigned colb = (unsigned)(cq * 128 + w * 64 + 4 * c) * 2u;
    v2u r0[8], r1[8], r2[8];
    const unsigned wob = EXP_NOTW ? OOB : (unsigned)(kvh * 512 + rb * 64 + 8 * g) * 1024u + colb;
#pragma unroll
    for (int i = 0; i < 8; ++i) r0[i] = __builtin_amdgcn_raw_buffer_load_b64(rwo, wob + i * 1024, 0, W_AUX);
#pragma unroll
    for (int i = 0; i < 8; ++i) r1[i] = __builtin_amdgcn_raw_buffer_load_b64(rwo, wob + 32768 + i * 1024, 0, W_AUX);
    const unsigned wdb = (unsigned)(pidx * 28 + 8 * g) * 1024u + colb;
#pragma unroll
    for (int i = 0; i < 8; ++i) r2[i] = __builtin_amdgcn_raw_buffer_load_b64(rwd, (8 * g + i < 28 && !EXP_NOTW) ? wdb + i * 1024 : OOB, 0, W_AUX);
    SB();
    tr8x4(r0, bo[0]); tr8x4(r1, bo[1]); tr8x4(r2, bd);
  } else if (w == 2) {
    const unsigned go = c < 14 ? WS_GU + (unsigned)((g * 896 + pidx * 14 + c) * 8) : OOB;
    v4u xv[4];
#pragma unroll
    for (int b = 0; b < 4; ++b) xv[b] = __builtin_amdgcn_raw_buffer_load_b128(rx, lane * 16 + b * 1024, 0, 0);
    const v4u gv = __builtin_amdgcn_raw_buffer_load_b128(rgw, lane * 16, 0, 0);
    v2u ga = {0, 0}, ua = {0, 0};
    for (unsigned spin = 0; spin < SPIN_CAP; ++spin) {
      CB();
      ga = __builtin_amdgcn_raw_buffer_load_b64(rws, go, 0, AUX_SC1);
      ua = __builtin_amdgcn_raw_buffer_load_b64(rws, go + 28672, 0, AUX_SC1);
      if (__builtin_amdgcn_ballot_w64(c >= 14 || (ga.y == tag && ua.y == tag)) == ~0ull) break;
      __builtin_amdgcn_s_sleep(2);
    }
    const float g0 = __uint_as_float(ga.x << 16), g1 = __uint_as_float(ga.x & 0xffff0000u), u0 = __uint_as_float(ua.x << 16), u1 = __uint_as_float(ua.x & 0xffff0000u);
    const float h0 = u0 / (1.f + __expf(-g0)), h1 = u1 / (1.f + __expf(-g1));
    *(LAS unsigned*)(L + L_H + (g * 16 + c) * 4) = c < 14 ? pk_bf16(h0, h1) : 0u;
    float a[4];
#pragma unroll
    for (int b = 0; b < 4; ++b) { a[b] = dot2(xv[b].x, gv.x, 0.f); a[b] = dot2(xv[b].y, gv.y, a[b]); a[b] = dot2(xv[b].z, gv.z, a[b]); a[b] = dot2(xv[b].w, gv.w, a[b]); }
    a[0] = swap32_add(a[0], a[2]); a[1] = swap32_add(a[1], a[3]);
    a[0] = swap16_add(a[0], a[1]);
    a[0] += dpp<ROW_ROR + 8>(a[0]); a[0] += dpp<ROW_ROR + 4>(a[0]); a[0] += dpp<ROW_ROR + 2>(a[0]); a[0] += dpp<ROW_ROR + 1>(a[0]);
    if (c == 0) *(LAS float*)(L + L_GATE + g * 4) = 1.f / (1.f + __expf(-a[0]));
  }
  BAR();
  {
    const int hd = w >> 1, dim = tid & 127;
    f32x4 ml[4]; float ov[8];
#pragma unroll
    for (int i = 0; i < 4; ++i) ml[i] = *(LAS f32x4*)(L + L_ML + hd * 64 + i * 16);
#pragma unroll
    for (int i = 0; i < 8; ++i) ov[i] = ((LAS float*)(L + L_MRG))[i * 512 + hd * 128 + dim];
    const float M = fmaxf(fmaxf(fmaxf(ml[0][0], ml[0][2]), fmaxf(ml[1][0], ml[1][2])), fmaxf(fmaxf(ml[2][0], ml[2][2]), fmaxf(ml[3][0], ml[3][2])));
    float Ls = 0.f, o = 0.f;
#pragma unroll
    for (int i = 0; i < 4; ++i) {
      const float w0 = __builtin_amdgcn_exp2f(ml[i][0] - M), w1 = __builtin_amdgcn_exp2f(ml[i][2] - M);
      Ls += w0 * ml[i][1] + w1 * ml[i][3]; o += w0 * ov[2 * i] + w1 * ov[2 * i + 1];
    }
    o *= __builtin_amdgcn_rcpf(Ls);
    const unsigned lseu = __float_as_uint(M + __builtin_amdgcn_logf(Ls));
    const unsigned nib = (lseu >> ((dim & 7) * 4)) & 15u;
    __builtin_amdgcn_raw_buffer_store_b32(emb(o, (t8 & 0x0fu) | (nib << 4)), rws, WS_PG + (unsigned)(((kvh * 32 + r) * 512 + tid) * 4), 0, AUX_SC1);
    SB();
  }
  STAMP(4);
  const f32x4 zero = {0.f, 0.f, 0.f, 0.f};
  f32x4 db[4];
  float gate = 0.f;
  if (w < 2) {
    const v4u hf = *(LAS v4u*)(L + L_H + ((c & 3) * 32 + 8 * g) * 2);
    gate = *(LAS float*)(L + L_GATE + g * 4);
#pragma unroll
    for (int e = 0; e < 4; ++e) db[e] = mfma32(hf, bd[e], zero);
  }
  {
    const int bb = w >> 1, sp = lane >> 3;
    const unsigned po = WS_PG + (unsigned)(((kvh * 32 + bb * 8 + sp) * 512 + rb * 64 + (w & 1) * 32 + (lane & 7) * 4) * 4);
    v4u pl = {0, 0, 0, 0};
    bool pok = false;
    for (unsigned spin = 0; spin < SPIN_CAP; ++spin) {
      CB();
      const v4u a = __builtin_amdgcn_raw_buffer_load_b128(rws, pok ? OOB : po, 0, AUX_SC1);
      const bool mine = (((a.x ^ t8) | (a.y ^ t8) | (a.z ^ t8) | (a.w ^ t8)) & 0x0fu) == 0u;
      const unsigned long long bm = __builtin_amdgcn_ballot_w64(mine);
      if (!pok && ((bm >> (lane & 56)) & 0xffull) == 0xffull) { pok = true; pl = a; }
      if (__builtin_amdgcn_ballot_w64(!pok) == 0ull) break;
      __builtin_amdgcn_s_sleep(SLP1);
    }
    unsigned half16 = ((pl.x >> 4) & 15u) | (pl.y & 0xf0u) | ((pl.z & 0xf0u) << 4) | ((pl.w & 0xf0u) << 8);
    const unsigned other = __builtin_amdgcn_mov_dpp(half16, 0xB1, 0xf, 0xf, true);
    const float lse = __uint_as_float((lane & 1) ? (other | (half16 << 16)) : (half16 | (other << 16)));
    float M = fmaxf(lse, dpp<ROW_ROR + 8>(lse)); M = xrow_max(M);
    const float wt_ = __builtin_amdgcn_exp2f(lse - M);
    float v0 = wt_ * unemb(pl.x), v1 = wt_ * unemb(pl.y), v2 = wt_ * unemb(pl.z), v3 = wt_ * unemb(pl.w), W = wt_;
    W += dpp<ROW_ROR + 8>(W); W = xrow_sum(W);
    v0 = swap32_add(v0, v2); v1 = swap32_add(v1, v3);
    v0 = swap16_add(v0, v0); v1 = swap16_add(v1, v1);
    v0 += dpp<ROW_ROR + 8>(v0); v1 += dpp<ROW_ROR + 8>(v1);
    const float inv = __builtin_amdgcn_rcpf(W);
    if ((lane & 24) == 0) *(LAS unsigned*)(L + L_O + (bb * 64 + (w & 1) * 32 + (lane & 7) * 4 + (lane >> 5) * 2) * 2) = pk_bf16(v0 * inv, v1 * inv);
  }
  BAR();
  STAMP(5);
  if (w < 2) {
    const v4u of0 = *(LAS v4u*)(L + L_O + ((c & 3) * 64 + 8 * g) * 2), of1 = *(LAS v4u*)(L + L_O + ((c & 3) * 64 + 32 + 8 * g) * 2);
    v4u res;
#pragma unroll
    for (int e = 0; e < 4; ++e) {
      f32x4 da = mfma32(of0, bo[0][e], zero);
      da = mfma32(of1, bo[1][e], da);
      res[e] = emb(gate * sel4(da, g) + (1.f - gate) * sel4(db[e], g), t8);
    }
    const int R = g * 16 + cq * 4 + w * 2 + (c >> 3);
    __builtin_amdgcn_raw_buffer_store_b128(res, rws, WS_FG + (unsigned)(((R * 64 + pidx) * 32 + (c & 7) * 4) * 4), 0, AUX_SC1);
    SB();
  }
  STAMP(6);
  if ((r & 3) == 0) {
    const int R = (r >> 2) * 8 + kvh;
    const unsigned fo = WS_FG + (unsigned)(R * 8192 + w * 1024 + lane * 16);
    v4u pl = {0, 0, 0, 0};
    bool ok = false;
    for (unsigned spin = 0; spin < SPIN_CAP; ++spin) {
      CB();
      const v4u a = __builtin_amdgcn_raw_buffer_load_b128(rws, ok ? OOB : fo, 0, AUX_SC1);
      if (!ok && emb_ok(a, t8)) { ok = true; pl = a; }
      if (__builtin_amdgcn_ballot_w64(!ok) == 0ull) break;
      __builtin_amdgcn_s_sleep(SLP2);
    }
    float v0 = unemb(pl.x), v1 = unemb(pl.y), v2 = unemb(pl.z), v3 = unemb(pl.w);
    v0 = swap32_add(v0, v2); v1 = swap32_add(v1, v3);
    v0 = swap16_add(v0, v0); v1 = swap16_add(v1, v1);
    v0 += dpp<ROW_ROR + 8>(v0); v1 += dpp<ROW_ROR + 8>(v1);
    if ((lane & 24) == 0) *(LAS f32x2*)(L + L_RED + (w * 32 + (lane & 7) * 4 + (lane >> 5) * 2) * 4) = f32x2{v0, v1};
    BAR();
    if (w == 0 && lane < 16) {
      const LAS f32x2* fr = (const LAS f32x2*)(L + L_RED) + lane;
      const f32x2 s = ((fr[0] + fr[16]) + (fr[32] + fr[48])) + ((fr[64] + fr[80]) + (fr[96] + fr[112]));
      ((unsigned*)pout)[R * 16 + lane] = pk_bf16(s.x, s.y);
      if (blk == 0 && lane == 0) __hip_atomic_store((unsigned*)pws, tag, __ATOMIC_RELAXED, __HIP_MEMORY_SCOPE_AGENT);
    }
  }
  STAMP(7);
}
}

extern "C" void kernel_launch(void* const* d_in, const int* in_sizes, int n_in, void* d_out, int out_size, void* d_ws,
                              size_t ws_size, hipStream_t stream) {
  fk::fused_kernel<<<256, 512, 0, stream>>>(d_in[0], d_in[1], d_in[2], d_in[3], d_in[4], d_in[6], d_in[7], d_in[8], d_in[9], d_out, d_ws);
}
```

```cpp
#include <hip/hip_runtime.h>
#include <cstdint>
#include <cstdio>

#define K_AUX 2
#define V_AUX 2
#define NBUF 5
#define STAMPS 0
#define W_AUX 2
#define QPRE 4
#define PREK 4
#define PREV 4
#define EXP_NOTILE 0
#define EXP_NOTW 0
#define EXP_NOX 0
#define EXP_NOQ 0
#define X_AUX 0
#define SLP1 2
#define SLP2 2
#define SPEC 2

typedef unsigned v4u __attribute__((ext_vector_type(4)));
typedef unsigned v2u __attribute__((ext_vector_type(2)));
typedef float f32x4 __attribute__((ext_vector_type(4)));
typedef float f32x2 __attribute__((ext_vector_type(2)));
typedef short s16x4 __attribute__((ext_vector_type(4)));
typedef __bf16 bf16x8 __attribute__((ext_vector_type(8)));
typedef __bf16 bf16x2 __attribute__((ext_vector_type(2)));
typedef __amdgpu_buffer_rsrc_t rsrc_t;
#define LAS __attribute__((address_space(3)))

namespace fk {
constexpr int AUX_NT = 2, AUX_SC1 = 16;
constexpr unsigned OOB = 0x40000000u;
constexpr unsigned WS_QG = 4096;
constexpr unsigned WS_GU = WS_QG + 65536;
constexpr unsigned WS_ST = WS_GU + 57344;
constexpr unsigned WS_PG = WS_ST + 8192;
constexpr unsigned WS_FG = WS_PG + 524288;
constexpr unsigned WS_END = WS_FG + 524288;
constexpr unsigned WS_STAMP = 8u << 20;
constexpr unsigned SPIN_CAP = 1u << 17;

constexpr int L_RED = 0;
constexpr int L_Q = 4096;
constexpr int L_H = L_Q + 1024;
constexpr int L_GATE = L_H + 256;
constexpr int L_FLAG = L_GATE + 16;
constexpr int L_O = L_FLAG + 16;
constexpr int L_ML = L_O + 512;
constexpr int L_MRG = L_ML + 256;
constexpr int L_END = L_MRG + 16384;

__device__ __forceinline__ rsrc_t mk(const void* p, unsigned bytes) {
  return __builtin_amdgcn_make_buffer_rsrc((void*)p, 0, (int)bytes, 0x00020000);
}
__device__ __forceinline__ float dot2(unsigned a, unsigned b, float c) {
  return __builtin_amdgcn_fdot2_f32_bf16(__builtin_bit_cast(bf16x2, a), __builtin_bit_cast(bf16x2, b), c, false);
}
__device__ __forceinline__ unsigned pk_bf16(float lo, float hi) {
  f32x2 v = {lo, hi};
  return __builtin_bit_cast(unsigned, __builtin_convertvector(v, bf16x2));
}
template <int CTRL> __device__ __forceinline__ float dpp(float x) {
  return __builtin_bit_cast(float, __builtin_amdgcn_mov_dpp(__builtin_bit_cast(int, x), CTRL, 0xf, 0xf, true));
}
__device__ __forceinline__ float swap32_add(float a, float b) {
  auto s = __builtin_amdgcn_permlane32_swap(__float_as_uint(a), __float_as_uint(b), false, false);
  return __uint_as_float(s[0]) + __uint_as_float(s[1]);
}
__device__ __forceinline__ float swap16_add(float a, float b) {
  auto s = __builtin_amdgcn_permlane16_swap(__float_as_uint(a), __float_as_uint(b), false, false);
  return __uint_as_float(s[0]) + __uint_as_float(s[1]);
}
__device__ __forceinline__ float xrow_max(float x) {
  auto s = __builtin_amdgcn_permlane16_swap(__float_as_uint(x), __float_as_uint(x), false, false);
  x = fmaxf(__uint_as_float(s[0]), __uint_as_float(s[1]));
  auto t = __builtin_amdgcn_permlane32_swap(__float_as_uint(x), __float_as_uint(x), false, false);
  return fmaxf(__uint_as_float(t[0]), __uint_as_float(t[1]));
}
__device__ __forceinline__ float xrow_sum(float x) {
  x = swap16_add(x, x);
  return swap32_add(x, x);
}
constexpr int ROW_SHL = 0x100, ROW_SHR = 0x110, ROW_ROR = 0x120;
__device__ __forceinline__ f32x2 treduce16(float (&v)[32], int lane) {
#pragma unroll
  for (int i = 0; i < 16; ++i) v[i] = swap32_add(v[i], v[i + 16]);
#pragma unroll
  for (int i = 0; i < 8; ++i) v[i] = swap16_add(v[i], v[i + 8]);
#pragma unroll
  for (int i = 0; i < 4; ++i) { float t1 = v[i] + dpp<ROW_ROR + 8>(v[i]), t2 = v[i + 4] + dpp<ROW_ROR + 8>(v[i + 4]); v[i] = (lane & 8) ? t2 : t1; }
#pragma unroll
  for (int i = 0; i < 2; ++i) { float t1 = v[i] + dpp<ROW_SHL + 4>(v[i]), t2 = v[i + 2] + dpp<ROW_SHR + 4>(v[i + 2]); v[i] = (lane & 4) ? t2 : t1; }
  return f32x2{v[0], v[1]};
}
template <bool FIRST>
__device__ __forceinline__ void rowpair(float (&v)[32], const v4u& w0, const v4u& w1, unsigned x0, unsigned x1, unsigned x2, unsigned x3) {
  const unsigned xp[4] = {x0, x1, x2, x3};
#pragma unroll
  for (int d = 0; d < 4; ++d) {
    const unsigned lo = __builtin_amdgcn_perm(w1[d], w0[d], 0x05040100u), hi = __builtin_amdgcn_perm(w1[d], w0[d], 0x07060302u);
#pragma unroll
    for (int b = 0; b < 4; ++b) {
      v[b * 8 + 2 * d] = dot2(lo, xp[b], FIRST ? 0.f : v[b * 8 + 2 * d]);
      v[b * 8 + 2 * d + 1] = dot2(hi, xp[b], FIRST ? 0.f : v[b * 8 + 2 * d + 1]);
    }
  }
}
__device__ __forceinline__ f32x4 mfma32(v4u a, v4u b, f32x4 c) {
  return __builtin_amdgcn_mfma_f32_16x16x32_bf16(__builtin_bit_cast(bf16x8, a), __builtin_bit_cast(bf16x8, b), c, 0, 0, 0);
}
__device__ __forceinline__ f32x4 mfma16(v2u a, v2u b, f32x4 c) {
  return __builtin_amdgcn_mfma_f32_16x16x16bf16_1k(__builtin_bit_cast(s16x4, a), __builtin_bit_cast(s16x4, b), c, 0, 0, 0);
}
__device__ __forceinline__ float sel4(const f32x4& d, int g) { return g == 0 ? d[0] : g == 1 ? d[1] : g == 2 ? d[2] : d[3]; }
__device__ __forceinline__ unsigned lds_inc(LAS unsigned char* p, int lane) {
  unsigned old = 0;
  if (lane == 0) old = __hip_atomic_fetch_add((LAS unsigned*)p, 1u, __ATOMIC_RELAXED, __HIP_MEMORY_SCOPE_WORKGROUP);
  return __builtin_amdgcn_readfirstlane(old);
}
__device__ __forceinline__ unsigned emb(float v, unsigned t8) { return ((__float_as_uint(v) + 0x80u) & 0xffffff00u) | t8; }
__device__ __forceinline__ bool emb_ok(const v4u& a, unsigned t8) { return (((a.x ^ t8) | (a.y ^ t8) | (a.z ^ t8) | (a.w ^ t8)) & 0xffu) == 0u; }
__device__ __forceinline__ float unemb(unsigned u) { return __uint_as_float(u & 0xffffff00u); }
__device__ __forceinline__ void tr8x4(const v2u (&r)[8], v4u (&f)[4]) {
#pragma unroll
  for (int i = 0; i < 4; ++i) {
    f[0][i] = __builtin_amdgcn_perm(r[2 * i + 1].x, r[2 * i].x, 0x05040100u); f[1][i] = __builtin_amdgcn_perm(r[2 * i + 1].x, r[2 * i].x, 0x07060302u);
    f[2][i] = __builtin_amdgcn_perm(r[2 * i + 1].y, r[2 * i].y, 0x05040100u); f[3][i] = __builtin_amdgcn_perm(r[2 * i + 1].y, r[2 * i].y, 0x07060302u);
  }
}
#define SB() __builtin_amdgcn_sched_barrier(0)
#define CB() asm volatile("" ::: "memory")
#define LGKM0() asm volatile("s_waitcnt lgkmcnt(0)" ::: "memory")
#define BAR() do { LGKM0(); __builtin_amdgcn_s_barrier(); CB(); } while (0)
#define STAMP(i) do { } while (0)

__global__ void __launch_bounds__(512, 2)
fused_kernel(const void* __restrict__ px, const void* __restrict__ pk, const void* __restrict__ pv, const void* __restrict__ pwq,
             const void* __restrict__ pwo, const void* __restrict__ pwg, const void* __restrict__ pwu, const void* __restrict__ pwd,
             const void* __restrict__ pgw, void* __restrict__ pout, void* pws) {
  __shared__ __attribute__((aligned(16))) unsigned char smem[L_END];
  LAS unsigned char* L = (LAS unsigned char*)smem;
  STAMP(0);
  asm volatile("" :: "s"(px), "s"(pk), "s"(pv), "s"(pwq), "s"(pwo), "s"(pwg), "s"(pwu), "s"(pwd), "s"(pgw), "s"(pout), "s"(pws));
  const int tid = threadIdx.x, lane = tid & 63, w = __builtin_amdgcn_readfirstlane(tid >> 6);
  const int g = lane >> 4, c = lane & 15;
  const int blk = blockIdx.x, kvh = blk & 7, r = blk >> 3;
  const int b_att = r >> 3, s_att = r & 7;
  const int rb = r >> 2, cq = r & 3;
  const int pidx = kvh * 8 + rb;

  const size_t kvoff = ((size_t)(b_att * 8 + kvh) * 8192 + s_att * 1024) * 256;
  const rsrc_t rk = mk((const char*)pk + kvoff, 262144), rv = mk((const char*)pv + kvoff, 262144);
  const rsrc_t rws = mk(pws, WS_END);
  const rsrc_t rx = mk(px, 4096), rgw = mk(pgw, 1024);
  const rsrc_t rwo = mk(pwo, 4096 * 512 * 2), rwd = mk(pwd, 1792 * 512 * 2);

  __builtin_amdgcn_s_setprio(3);
  const int rr4 = lane >> 2, ch = lane & 3;
  const bool isq = r < 16;
  const int um = (r - 16) >= 7 ? 1 : 0;
  const int ucb = kvh * 7 + (r - 16) - 7 * um;
  v4u wt[4]; v2u xq[4];
  {
    const int row = w * 64 + 4 * rr4;
    const unsigned rstr = isq ? 8192u : 3584u;
    const rsrc_t ru = mk(isq ? pwq : (um ? pwu : pwg), isq ? 512 * 4096 * 2 : 512 * 1792 * 2);
    const unsigned uo = (r < 30 && !EXP_NOTILE) ? (unsigned)row * rstr + (unsigned)((isq ? kvh * 16 + r : ucb) * 32 + ch * 8) * 2u : OOB;
#pragma unroll
    for (int i = 0; i < 4; ++i) wt[i] = __builtin_amdgcn_raw_buffer_load_b128(ru, uo + i * rstr, 0, W_AUX);
#pragma unroll
    for (int b = 0; b < 4; ++b) xq[b] = __builtin_amdgcn_raw_buffer_load_b64(rx, EXP_NOX ? OOB : row * 2 + b * 1024, 0, X_AUX);
  }
  SB();
  if (tid == 0) { *(volatile LAS v4u*)(L + L_FLAG) = v4u{0u, 0u, 0u, 0u}; }
  BAR();
  SB();
  const unsigned ko = c * 256 + g * 16, vo = g * 1024 + c * 16;
  v4u kb[NBUF][4], vb[NBUF][4];
  auto issueK = [&](int j, int i0, int i1) {
    const unsigned so = (unsigned)(j * 8 + w) * 4096u;
#pragma unroll
    for (int i = i0; i < i1; ++i) kb[j % NBUF][i] = __builtin_amdgcn_raw_buffer_load_b128(rk, ko + i * 64, so, K_AUX);
    SB();
  };
  auto issueV = [&](int j, int i0, int i1) {
    const unsigned so = (unsigned)(j * 8 + w) * 4096u;
#pragma unroll
    for (int i = i0; i < i1; ++i) vb[j % NBUF][i] = __builtin_amdgcn_raw_buffer_load_b128(rv, vo + i * 256, so, V_AUX);
    SB();
  };
  auto issue = [&](int j) { issueK(j, 0, 4); issueV(j, 0, 4); STAMP(11 + j); };
  const unsigned E = *(const __attribute__((address_space(4))) unsigned*)pws;
  SB();
  issueV(0, 0, PREV);
  issueK(0, 0, PREK);
  const unsigned tag = E * 0x9E3779B5u + 0x7F4A7C15u;
  const unsigned t8 = tag & 0xffu;
  LAS f32x2* red = (LAS f32x2*)(L + L_RED);
  {
    float v[32];
    rowpair<true>(v, wt[0], wt[1], xq[0].x, xq[1].x, xq[2].x, xq[3].x);
    rowpair<false>(v, wt[2], wt[3], xq[0].y, xq[1].y, xq[2].y, xq[3].y);
    red[w * 64 + lane] = treduce16(v, lane);
    LGKM0();
    const unsigned old = lds_inc(L + L_FLAG + 4, lane);
    CB();
    const f32x2 s = ((red[lane] + red[64 + lane]) + (red[128 + lane] + red[192 + lane])) + ((red[256 + lane] + red[320 + lane]) + (red[384 + lane] + red[448 + lane]));
    const int pr = ch * 4 + (rr4 & 3);
    const unsigned po = isq ? WS_QG + (unsigned)(((kvh * 4 + g) * 256 + r * 16 + pr) * 8) : WS_GU + (unsigned)(((um * 4 + g) * 896 + ucb * 16 + pr) * 8);
    __builtin_amdgcn_raw_buffer_store_b64(v2u{pk_bf16(s.x, s.y), tag}, rws, (old == 7 && r < 30) ? po : OOB, 0, AUX_SC1);
    SB();
    __builtin_amdgcn_s_setprio(0);
    SB();
  }
  STAMP(1);
  issueV(0, PREV, 4);
  issueK(0, PREK, 4);
  STAMP(11);
#pragma unroll
  for (int j = 1; j < QPRE; ++j) issue(j);
  const unsigned qgo = (w < 2 && !EXP_NOQ) ? WS_QG + (unsigned)((kvh * 4 + b_att) * 2048 + w * 1024 + lane * 16) : OOB;
  CB();
  v4u qa = __builtin_amdgcn_raw_buffer_load_b128(rws, qgo, 0, AUX_SC1);
  SB();
  issue(QPRE);
  if (w < 2) {
    if (!EXP_NOQ && __builtin_amdgcn_ballot_w64(qa.y == tag && qa.w == tag) != ~0ull) {
      STAMP(10);
      for (unsigned spin = 0; spin < SPIN_CAP; ++spin) {
        CB();
        qa = __builtin_amdgcn_raw_buffer_load_b128(rws, qgo, 0, AUX_SC1);
        if (__builtin_amdgcn_ballot_w64(qa.y == tag && qa.w == tag) == ~0ull) break;
      }
    }
    *(LAS v2u*)(L + L_Q + (w * 128 + lane * 2) * 4) = v2u{qa.x, qa.z};
    LGKM0();
    (void)lds_inc(L + L_FLAG, lane);
    STAMP(9);
  }
  SB();
#pragma unroll
  for (int j = QPRE + 1; j < NBUF; ++j) issue(j);
  for (unsigned spin = 0; spin < (SPIN_CAP << 4); ++spin) {
    if (*(volatile LAS unsigned*)(L + L_FLAG) >= 2u) break;
    __builtin_amdgcn_s_sleep(1);
  }
  CB();
  STAMP(2);
  v4u qf[4];
#pragma unroll
  for (int i = 0; i < 4; ++i) qf[i] = *(LAS v4u*)(L + L_Q + (((c & 3) * 64 + 16 * i + 4 * g) * 4));

  constexpr float C2 = 0.08838834764831845f * 1.4426950408889634f;
  f32x4 acc[8];
#pragma unroll
  for (int e = 0; e < 8; ++e) acc[e] = f32x4{0.f, 0.f, 0.f, 0.f};
  float m = -INFINITY, l = 0.f;
#pragma unroll
  for (int j = 0; j < 8; ++j) {
    const int bf = j % NBUF;
    f32x4 sc = {0.f, 0.f, 0.f, 0.f};
#pragma unroll
    for (int i = 0; i < 4; ++i) sc = mfma32(kb[bf][i], qf[i], sc);
    const float t0 = sc[0] * C2, t1 = sc[1] * C2, t2 = sc[2] * C2, t3 = sc[3] * C2;
    const float cm = xrow_max(fmaxf(fmaxf(t0, t1), fmaxf(t2, t3)));
    const float mn = fmaxf(m, cm);
    const float alpha = __builtin_amdgcn_exp2f(m - mn);
    const float p0 = __builtin_amdgcn_exp2f(t0 - mn), p1 = __builtin_amdgcn_exp2f(t1 - mn), p2 = __builtin_amdgcn_exp2f(t2 - mn), p3 = __builtin_amdgcn_exp2f(t3 - mn);
    l = l * alpha + ((p0 + p1) + (p2 + p3));
    m = mn;
    const v2u pa = {pk_bf16(p0, p1), pk_bf16(p2, p3)};
    if (j > 0) {
      const float a0 = dpp<0x00>(alpha), a1 = dpp<0x55>(alpha), a2 = dpp<0xAA>(alpha), a3 = dpp<0xFF>(alpha);
#pragma unroll
      for (int e = 0; e < 8; ++e) { acc[e][0] *= a0; acc[e][1] *= a1; acc[e][2] *= a2; acc[e][3] *= a3; }
    }
#pragma unroll
    for (int d = 0; d < 4; ++d) {
      const v2u lo = {__builtin_amdgcn_perm(vb[bf][1][d], vb[bf][0][d], 0x05040100u), __builtin_amdgcn_perm(vb[bf][3][d], vb[bf][2][d], 0x05040100u)};
      const v2u hi = {__builtin_amdgcn_perm(vb[bf][1][d], vb[bf][0][d], 0x07060302u), __builtin_amdgcn_perm(vb[bf][3][d], vb[bf][2][d], 0x07060302u)};
      acc[2 * d] = mfma16(pa, lo, acc[2 * d]);
      acc[2 * d + 1] = mfma16(pa, hi, acc[2 * d + 1]);
    }
    SB();
    if (j + NBUF < 8) issue(j + NBUF);
  }
  STAMP(3);
  l = xrow_sum(l);
  if (g == 0) {
    LAS float* mo = (LAS float*)(L + L_MRG) + w * 512 + 8 * c;
#pragma unroll
    for (int h = 0; h < 4; ++h) {
      *(LAS f32x4*)(mo + h * 128) = f32x4{acc[0][h], acc[1][h], acc[2][h], acc[3][h]};
      *(LAS f32x4*)(mo + h * 128 + 4) = f32x4{acc[4][h], acc[5][h], acc[6][h], acc[7][h]};
    }
    if (c < 4) *(LAS f32x2*)(L + L_ML + (c * 8 + w) * 8) = f32x2{m, l};
  }
  v4u bo[2][4], bd[4];
  if (w < 2) {
    const unsigned colb = (unsigned)(cq * 128 + w * 64 + 4 * c) * 2u;
    v2u r0[8], r1[8], r2[8];
    const unsigned wob = EXP_NOTW ? OOB : (unsigned)(kvh * 512 + rb * 64 + 8 * g) * 1024u + colb;
#pragma unroll
    for (int i = 0; i < 8; ++i) r0[i] = __builtin_amdgcn_raw_buffer_load_b64(rwo, wob + i * 1024, 0, W_AUX);
#pragma unroll
    for (int i = 0; i < 8; ++i) r1[i] = __builtin_amdgcn_raw_buffer_load_b64(rwo, wob + 32768 + i * 1024, 0, W_AUX);
    const unsigned wdb = (unsigned)(pidx * 28 + 8 * g) * 1024u + colb;
#pragma unroll
    for (int i = 0; i < 8; ++i) r2[i] = __builtin_amdgcn_raw_buffer_load_b64(rwd, (8 * g + i < 28 && !EXP_NOTW) ? wdb + i * 1024 : OOB, 0, W_AUX);
    SB();
    tr8x4(r0, bo[0]); tr8x4(r1, bo[1]); tr8x4(r2, bd);
  } else if (w == 2) {
    const unsigned go = c < 14 ? WS_GU + (unsigned)((g * 896 + pidx * 14 + c) * 8) : OOB;
    v4u xv[4];
#pragma unroll
    for (int b = 0; b < 4; ++b) xv[b] = __builtin_amdgcn_raw_buffer_load_b128(rx, lane * 16 + b * 1024, 0, 0);
    const v4u gv = __builtin_amdgcn_raw_buffer_load_b128(rgw, lane * 16, 0, 0);
    v2u ga = {0, 0}, ua = {0, 0};
    for (unsigned spin = 0; spin < SPIN_CAP; ++spin) {
      CB();
      ga = __builtin_amdgcn_raw_buffer_load_b64(rws, go, 0, AUX_SC1);
      ua = __builtin_amdgcn_raw_buffer_load_b64(rws, go + 28672, 0, AUX_SC1);
      if (__builtin_amdgcn_ballot_w64(c >= 14 || (ga.y == tag && ua.y == tag)) == ~0ull) break;
      __builtin_amdgcn_s_sleep(2);
    }
    const float g0 = __uint_as_float(ga.x << 16), g1 = __uint_as_float(ga.x & 0xffff0000u), u0 = __uint_as_float(ua.x << 16), u1 = __uint_as_float(ua.x & 0xffff0000u);
    const float h0 = u0 / (1.f + __expf(-g0)), h1 = u1 / (1.f + __expf(-g1));
    *(LAS unsigned*)(L + L_H + (g * 16 + c) * 4) = c < 14 ? pk_bf16(h0, h1) : 0u;
    float a[4];
#pragma unroll
    for (int b = 0; b < 4; ++b) { a[b] = dot2(xv[b].x, gv.x, 0.f); a[b] = dot2(xv[b].y, gv.y, a[b]); a[b] = dot2(xv[b].z, gv.z, a[b]); a[b] = dot2(xv[b].w, gv.w, a[b]); }
    a[0] = swap32_add(a[0], a[2]); a[1] = swap32_add(a[1], a[3]);
    a[0] = swap16_add(a[0], a[1]);
    a[0] += dpp<ROW_ROR + 8>(a[0]); a[0] += dpp<ROW_ROR + 4>(a[0]); a[0] += dpp<ROW_ROR + 2>(a[0]); a[0] += dpp<ROW_ROR + 1>(a[0]);
    if (c == 0) *(LAS float*)(L + L_GATE + g * 4) = 1.f / (1.f + __expf(-a[0]));
  }
  BAR();
  {
    const int hd = w >> 1, dim = tid & 127;
    f32x4 ml[4]; float ov[8];
#pragma unroll
    for (int i = 0; i < 4; ++i) ml[i] = *(LAS f32x4*)(L + L_ML + hd * 64 + i * 16);
#pragma unroll
    for (int i = 0; i < 8; ++i) ov[i] = ((LAS float*)(L + L_MRG))[i * 512 + hd * 128 + dim];
    const float M = fmaxf(fmaxf(fmaxf(ml[0][0], ml[0][2]), fmaxf(ml[1][0], ml[1][2])), fmaxf(fmaxf(ml[2][0], ml[2][2]), fmaxf(ml[3][0], ml[3][2])));
    float Ls = 0.f, o = 0.f;
#pragma unroll
    for (int i = 0; i < 4; ++i) {
      const float w0 = __builtin_amdgcn_exp2f(ml[i][0] - M), w1 = __builtin_amdgcn_exp2f(ml[i][2] - M);
      Ls += w0 * ml[i][1] + w1 * ml[i][3]; o += w0 * ov[2 * i] + w1 * ov[2 * i + 1];
    }
    o *= __builtin_amdgcn_rcpf(Ls);
    const unsigned lseu = __float_as_uint(M + __builtin_amdgcn_logf(Ls));
    const unsigned nib = (lseu >> ((dim & 7) * 4)) & 15u;
    __builtin_amdgcn_raw_buffer_store_b32(emb(o, (t8 & 0x0fu) | (nib << 4)), rws, WS_PG + (unsigned)(((kvh * 32 + r) * 512 + tid) * 4), 0, AUX_SC1);
    SB();
  }
  STAMP(4);
  const f32x4 zero = {0.f, 0.f, 0.f, 0.f};
  f32x4 db[4];
  float gate = 0.f;
  if (w < 2) {
    const v4u hf = *(LAS v4u*)(L + L_H + ((c & 3) * 32 + 8 * g) * 2);
    gate = *(LAS float*)(L + L_GATE + g * 4);
#pragma unroll
    for (int e = 0; e < 4; ++e) db[e] = mfma32(hf, bd[e], zero);
  }
  {
    const int bb = w >> 1, sp = lane >> 3;
    const unsigned po = WS_PG + (unsigned)(((kvh * 32 + bb * 8 + sp) * 512 + rb * 64 + (w & 1) * 32 + (lane & 7) * 4) * 4);
    v4u pl = {0, 0, 0, 0};
    bool pok = false;
    for (unsigned spin = 0; spin < SPIN_CAP; ++spin) {
      CB();
      const v4u a = __builtin_amdgcn_raw_buffer_load_b128(rws, pok ? OOB : po, 0, AUX_SC1);
      const bool mine = (((a.x ^ t8) | (a.y ^ t8) | (a.z ^ t8) | (a.w ^ t8)) & 0x0fu) == 0u;
      const unsigned long long bm = __builtin_amdgcn_ballot_w64(mine);
      if (!pok && ((bm >> (lane & 56)) & 0xffull) == 0xffull) { pok = true; pl = a; }
      if (__builtin_amdgcn_ballot_w64(!pok) == 0ull) break;
      __builtin_amdgcn_s_sleep(SLP1);
    }
    unsigned half16 = ((pl.x >> 4) & 15u) | (pl.y & 0xf0u) | ((pl.z & 0xf0u) << 4) | ((pl.w & 0xf0u) << 8);
    const unsigned other = __builtin_amdgcn_mov_dpp(half16, 0xB1, 0xf, 0xf, true);
    const float lse = __uint_as_float((lane & 1) ? (other | (half16 << 16)) : (half16 | (other << 16)));
    float M = fmaxf(lse, dpp<ROW_ROR + 8>(lse)); M = xrow_max(M);
    const float wt_ = __builtin_amdgcn_exp2f(lse - M);
    float v0 = wt_ * unemb(pl.x), v1 = wt_ * unemb(pl.y), v2 = wt_ * unemb(pl.z), v3 = wt_ * unemb(pl.w), W = wt_;
    W += dpp<ROW_ROR + 8>(W); W = xrow_sum(W);
    v0 = swap32_add(v0, v2); v1 = swap32_add(v1, v3);
    v0 = swap16_add(v0, v0); v1 = swap16_add(v1, v1);
    v0 += dpp<ROW_ROR + 8>(v0); v1 += dpp<ROW_ROR + 8>(v1);
    const float inv = __builtin_amdgcn_rcpf(W);
    if ((lane & 24) == 0) *(LAS unsigned*)(L + L_O + (bb * 64 + (w & 1) * 32 + (lane & 7) * 4 + (lane >> 5) * 2) * 2) = pk_bf16(v0 * inv, v1 * inv);
  }
  BAR();
  STAMP(5);
  if (w < 2) {
    const v4u of0 = *(LAS v4u*)(L + L_O + ((c & 3) * 64 + 8 * g) * 2), of1 = *(LAS v4u*)(L + L_O + ((c & 3) * 64 + 32 + 8 * g) * 2);
    v4u res;
#pragma unroll
    for (int e = 0; e < 4; ++e) {
      f32x4 da = mfma32(of0, bo[0][e], zero);
      da = mfma32(of1, bo[1][e], da);
      res[e] = emb(gate * sel4(da, g) + (1.f - gate) * sel4(db[e], g), t8);
    }
    const int R = g * 16 + cq * 4 + w * 2 + (c >> 3);
    __builtin_amdgcn_raw_buffer_store_b128(res, rws, WS_FG + (unsigned)(((R * 64 + pidx) * 32 + (c & 7) * 4) * 4), 0, AUX_SC1);
    SB();
  }
  STAMP(6);
  if ((r & 3) == 0) {
    const int R = (r >> 2) * 8 + kvh;
    const unsigned fo = WS_FG + (unsigned)(R * 8192 + w * 1024 + lane * 16);
    v4u pl = {0, 0, 0, 0};
    bool ok = false;
    for (unsigned spin = 0; spin < SPIN_CAP; ++spin) {
      CB();
      const v4u a = __builtin_amdgcn_raw_buffer_load_b128(rws, ok ? OOB : fo, 0, AUX_SC1);
      if (!ok && emb_ok(a, t8)) { ok = true; pl = a; }
      if (__builtin_amdgcn_ballot_w64(!ok) == 0ull) break;
      __builtin_amdgcn_s_sleep(SLP2);
    }
    float v0 = unemb(pl.x), v1 = unemb(pl.y), v2 = unemb(pl.z), v3 = unemb(pl.w);
    v0 = swap32_add(v0, v2); v1 = swap32_add(v1, v3);
    v0 = swap16_add(v0, v0); v1 = swap16_add(v1, v1);
    v0 += dpp<ROW_ROR + 8>(v0); v1 += dpp<ROW_ROR + 8>(v1);
    if ((lane & 24) == 0) *(LAS f32x2*)(L + L_RED + (w * 32 + (lane & 7) * 4 + (lane >> 5) * 2) * 4) = f32x2{v0, v1};
    BAR();
    if (w == 0 && lane < 16) {
      const LAS f32x2* fr = (const LAS f32x2*)(L + L_RED) + lane;
      const f32x2 s = ((fr[0] + fr[16]) + (fr[32] + fr[48])) + ((fr[64] + fr[80]) + (fr[96] + fr[112]));
      ((unsigned*)pout)[R * 16 + lane] = pk_bf16(s.x, s.y);
      if (blk == 0 && lane == 0) __hip_atomic_store((unsigned*)pws, tag, __ATOMIC_RELAXED, __HIP_MEMORY_SCOPE_AGENT);
    }
  }
  STAMP(7);
}
}

extern "C" void kernel_launch(void* const* d_in, const int* in_sizes, int n_in, void* d_out, int out_size, void* d_ws,
                              size_t ws_size, hipStream_t stream) {
  for (int i = 0; i < n_in; ++i) { hipPointerAttribute_t a; hipError_t e = hipPointerGetAttributes(&a, d_in[i]); void* base = 0; size_t sz = 0; hipError_t e2 = hipMemGetAddressRange((hipDeviceptr_t*)&base, &sz, (hipDeviceptr_t)d_in[i]);
    fprintf(stderr, "in%d %p type %d flags %u managed %d err %d | base %p size %zu err %d\n", i, d_in[i], (int)a.type, a.allocationFlags, a.isManaged, (int)e, base, sz, (int)e2); }
  { void* base = 0; size_t sz = 0; hipMemGetAddressRange((hipDeviceptr_t*)&base, &sz, (hipDeviceptr_t)d_ws); fprintf(stderr, "out %p ws %p (base %p size %zu)\n", d_out, d_ws, base, sz); }
  fk::fused_kernel<<<256, 512, 0, stream>>>(d_in[0], d_in[1], d_in[2], d_in[3], d_in[4], d_in[6], d_in[7], d_in[8], d_in[9], d_out, d_ws);
}
```

```cpp
#define DIAG 0
#define DMUL 1
#include <hip/hip_runtime.h>
#include <cstdint>

#define K_AUX 2
#define V_AUX 2
#define NBUF 5
#define STAMPS 0
#define W_AUX 2
#define QPRE 4
#define PREK 4
#define PREV 4
#define EXP_NOTILE 0
#define EXP_NOTW 0
#define EXP_NOX 0
#define EXP_NOQ 0
#define X_AUX 0
#define SLP1 2
#define SLP2 2
#define SPEC 2

typedef unsigned v4u __attribute__((ext_vector_type(4)));
typedef unsigned v2u __attribute__((ext_vector_type(2)));
typedef float f32x4 __attribute__((ext_vector_type(4)));
typedef float f32x2 __attribute__((ext_vector_type(2)));
typedef short s16x4 __attribute__((ext_vector_type(4)));
typedef __bf16 bf16x8 __attribute__((ext_vector_type(8)));
typedef __bf16 bf16x2 __attribute__((ext_vector_type(2)));
typedef __amdgpu_buffer_rsrc_t rsrc_t;
#define LAS __attribute__((address_space(3)))

namespace fk {
constexpr int AUX_NT = 2, AUX_SC1 = 16;
constexpr unsigned OOB = 0x40000000u;
constexpr unsigned WS_QG = 4096;
constexpr unsigned WS_GU = WS_QG + 65536;
constexpr unsigned WS_ST = WS_GU + 57344;
constexpr unsigned WS_PG = WS_ST + 8192;
constexpr unsigned WS_FG = WS_PG + 524288;
constexpr unsigned WS_END = WS_FG + 524288;
constexpr unsigned WS_STAMP = 8u << 20;
constexpr unsigned SPIN_CAP = 1u << 17;

constexpr int L_RED = 0;
constexpr int L_Q = 4096;
constexpr int L_H = L_Q + 1024;
constexpr int L_GATE = L_H + 256;
constexpr int L_FLAG = L_GATE + 16;
constexpr int L_O = L_FLAG + 16;
constexpr int L_ML = L_O + 512;
constexpr int L_MRG = L_ML + 256;
constexpr int L_END = L_MRG + 16384;

__device__ __forceinline__ rsrc_t mk(const void* p, unsigned bytes) {
  return __builtin_amdgcn_make_buffer_rsrc((void*)p, 0, (int)bytes, 0x00020000);
}
__device__ __forceinline__ float dot2(unsigned a, unsigned b, float c) {
  return __builtin_amdgcn_fdot2_f32_bf16(__builtin_bit_cast(bf16x2, a), __builtin_bit_cast(bf16x2, b), c, false);
}
__device__ __forceinline__ unsigned pk_bf16(float lo, float hi) {
  f32x2 v = {lo, hi};
  return __builtin_bit_cast(unsigned, __builtin_convertvector(v, bf16x2));
}
template <int CTRL> __device__ __forceinline__ float dpp(float x) {
  return __builtin_bit_cast(float, __builtin_amdgcn_mov_dpp(__builtin_bit_cast(int, x), CTRL, 0xf, 0xf, true));
}
__device__ __forceinline__ float swap32_add(float a, float b) {
  auto s = __builtin_amdgcn_permlane32_swap(__float_as_uint(a), __float_as_uint(b), false, false);
  return __uint_as_float(s[0]) + __uint_as_float(s[1]);
}
__device__ __forceinline__ float swap16_add(float a, float b) {
  auto s = __builtin_amdgcn_permlane16_swap(__float_as_uint(a), __float_as_uint(b), false, false);
  return __uint_as_float(s[0]) + __uint_as_float(s[1]);
}
__device__ __forceinline__ float xrow_max(float x) {
  auto s = __builtin_amdgcn_permlane16_swap(__float_as_uint(x), __float_as_uint(x), false, false);
  x = fmaxf(__uint_as_float(s[0]), __uint_as_float(s[1]));
  auto t = __builtin_amdgcn_permlane32_swap(__float_as_uint(x), __float_as_uint(x), false, false);
  return fmaxf(__uint_as_float(t[0]), __uint_as_float(t[1]));
}
__device__ __forceinline__ float xrow_sum(float x) {
  x = swap16_add(x, x);
  return swap32_add(x, x);
}
constexpr int ROW_SHL = 0x100, ROW_SHR = 0x110, ROW_ROR = 0x120;
__device__ __forceinline__ f32x2 treduce16(float (&v)[32], int lane) {
#pragma unroll
  for (int i = 0; i < 16; ++i) v[i] = swap32_add(v[i], v[i + 16]);
#pragma unroll
  for (int i = 0; i < 8; ++i) v[i] = swap16_add(v[i], v[i + 8]);
#pragma unroll
  for (int i = 0; i < 4; ++i) { float t1 = v[i] + dpp<ROW_ROR + 8>(v[i]), t2 = v[i + 4] + dpp<ROW_ROR + 8>(v[i + 4]); v[i] = (lane & 8) ? t2 : t1; }
#pragma unroll
  for (int i = 0; i < 2; ++i) { float t1 = v[i] + dpp<ROW_SHL + 4>(v[i]), t2 = v[i + 2] + dpp<ROW_SHR + 4>(v[i + 2]); v[i] = (lane & 4) ? t2 : t1; }
  return f32x2{v[0], v[1]};
}
template <bool FIRST>
__device__ __forceinline__ void rowpair(float (&v)[32], const v4u& w0, const v4u& w1, unsigned x0, unsigned x1, unsigned x2, unsigned x3) {
  const unsigned xp[4] = {x0, x1, x2, x3};
#pragma unroll
  for (int d = 0; d < 4; ++d) {
    const unsigned lo = __builtin_amdgcn_perm(w1[d], w0[d], 0x05040100u), hi = __builtin_amdgcn_perm(w1[d], w0[d], 0x07060302u);
#pragma unroll
    for (int b = 0; b < 4; ++b) {
      v[b * 8 + 2 * d] = dot2(lo, xp[b], FIRST ? 0.f : v[b * 8 + 2 * d]);
      v[b * 8 + 2 * d + 1] = dot2(hi, xp[b], FIRST ? 0.f : v[b * 8 + 2 * d + 1]);
    }
  }
}
__device__ __forceinline__ f32x4 mfma32(v4u a, v4u b, f32x4 c) {
  return __builtin_amdgcn_mfma_f32_16x16x32_bf16(__builtin_bit_cast(bf16x8, a), __builtin_bit_cast(bf16x8, b), c, 0, 0, 0);
}
__device__ __forceinline__ f32x4 mfma16(v2u a, v2u b, f32x4 c) {
  return __builtin_amdgcn_mfma_f32_16x16x16bf16_1k(__builtin_bit_cast(s16x4, a), __builtin_bit_cast(s16x4, b), c, 0, 0, 0);
}
__device__ __forceinline__ float sel4(const f32x4& d, int g) { return g == 0 ? d[0] : g == 1 ? d[1] : g == 2 ? d[2] : d[3]; }
__device__ __forceinline__ unsigned lds_inc(LAS unsigned char* p, int lane) {
  unsigned old = 0;
  if (lane == 0) old = __hip_atomic_fetch_add((LAS unsigned*)p, 1u, __ATOMIC_RELAXED, __HIP_MEMORY_SCOPE_WORKGROUP);
  return __builtin_amdgcn_readfirstlane(old);
}
__device__ __forceinline__ unsigned emb(float v, unsigned t8) { return ((__float_as_uint(v) + 0x80u) & 0xffffff00u) | t8; }
__device__ __forceinline__ bool emb_ok(const v4u& a, unsigned t8) { return (((a.x ^ t8) | (a.y ^ t8) | (a.z ^ t8) | (a.w ^ t8)) & 0xffu) == 0u; }
__device__ __forceinline__ float unemb(unsigned u) { return __uint_as_float(u & 0xffffff00u); }
__device__ __forceinline__ void tr8x4(const v2u (&r)[8], v4u (&f)[4]) {
#pragma unroll
  for (int i = 0; i < 4; ++i) {
    f[0][i] = __builtin_amdgcn_perm(r[2 * i + 1].x, r[2 * i].x, 0x05040100u); f[1][i] = __builtin_amdgcn_perm(r[2 * i + 1].x, r[2 * i].x, 0x07060302u);
    f[2][i] = __builtin_amdgcn_perm(r[2 * i + 1].y, r[2 * i].y, 0x05040100u); f[3][i] = __builtin_amdgcn_perm(r[2 * i + 1].y, r[2 * i].y, 0x07060302u);
  }
}
#define SB() __builtin_amdgcn_sched_barrier(0)
#define CB() asm volatile("" ::: "memory")
#define LGKM0() asm volatile("s_waitcnt lgkmcnt(0)" ::: "memory")
#define BAR() do { LGKM0(); __builtin_amdgcn_s_barrier(); CB(); } while (0)
#define STAMP(i) do { } while (0)

__global__ void __launch_bounds__(512, 2)
fused_kernel(const void* __restrict__ px, const void* __restrict__ pk, const void* __restrict__ pv, const void* __restrict__ pwq,
             const void* __restrict__ pwo, const void* __restrict__ pwg, const void* __restrict__ pwu, const void* __restrict__ pwd,
             const void* __restrict__ pgw, void* __restrict__ pout, void* pws) {
  __shared__ __attribute__((aligned(16))) unsigned char smem[L_END];
  LAS unsigned char* L = (LAS unsigned char*)smem;
  STAMP(0);
  const unsigned long long dT0 = __builtin_amdgcn_s_memrealtime(); unsigned long long dX = 0;
  asm volatile("" :: "s"(px), "s"(pk), "s"(pv), "s"(pwq), "s"(pwo), "s"(pwg), "s"(pwu), "s"(pwd), "s"(pgw), "s"(pout), "s"(pws));
  const int tid = threadIdx.x, lane = tid & 63, w = __builtin_amdgcn_readfirstlane(tid >> 6);
  const int g = lane >> 4, c = lane & 15;
  const int blk = blockIdx.x, kvh = blk & 7, r = blk >> 3;
  const int b_att = r >> 3, s_att = r & 7;
  const int rb = r >> 2, cq = r & 3;
  const int pidx = kvh * 8 + rb;

  const size_t kvoff = ((size_t)(b_att * 8 + kvh) * 8192 + s_att * 1024) * 256;
  const rsrc_t rk = mk((const char*)pk + kvoff, 262144), rv = mk((const char*)pv + kvoff, 262144);
  const rsrc_t rws = mk(pws, WS_END);
  const rsrc_t rx = mk(px, 4096), rgw = mk(pgw, 1024);
  const rsrc_t rwo = mk(pwo, 4096 * 512 * 2), rwd = mk(pwd, 1792 * 512 * 2);

  __builtin_amdgcn_s_setprio(3);
  const int rr4 = lane >> 2, ch = lane & 3;
  const bool isq = r < 16;
  const int um = (r - 16) >= 7 ? 1 : 0;
  const int ucb = kvh * 7 + (r - 16) - 7 * um;
  v4u wt[4]; v2u xq[4];
  {
    const int row = w * 64 + 4 * rr4;
    const unsigned rstr = isq ? 8192u : 3584u;
    const rsrc_t ru = mk(isq ? pwq : (um ? pwu : pwg), isq ? 512 * 4096 * 2 : 512 * 1792 * 2);
    const unsigned uo = (r < 30 && !EXP_NOTILE) ? (unsigned)row * rstr + (unsigned)((isq ? kvh * 16 + r : ucb) * 32 + ch * 8) * 2u : OOB;
#pragma unroll
    for (int i = 0; i < 4; ++i) wt[i] = __builtin_amdgcn_raw_buffer_load_b128(ru, uo + i * rstr, 0, W_AUX);
#pragma unroll
    for (int b = 0; b < 4; ++b) xq[b] = __builtin_amdgcn_raw_buffer_load_b64(rx, EXP_NOX ? OOB : row * 2 + b * 1024, 0, X_AUX);
  }
  SB();
  if (tid == 0) { *(volatile LAS v4u*)(L + L_FLAG) = v4u{0u, 0u, 0u, 0u}; }
  BAR();
  SB();
  const unsigned ko = c * 256 + g * 16, vo = g * 1024 + c * 16;
  v4u kb[NBUF][4], vb[NBUF][4];
  auto issueK = [&](int j, int i0, int i1) {
    const unsigned so = (unsigned)(j * 8 + w) * 4096u;
#pragma unroll
    for (int i = i0; i < i1; ++i) kb[j % NBUF][i] = __builtin_amdgcn_raw_buffer_load_b128(rk, ko + i * 64, so, K_AUX);
    SB();
  };
  auto issueV = [&](int j, int i0, int i1) {
    const unsigned so = (unsigned)(j * 8 + w) * 4096u;
#pragma unroll
    for (int i = i0; i < i1; ++i) vb[j % NBUF][i] = __builtin_amdgcn_raw_buffer_load_b128(rv, vo + i * 256, so, V_AUX);
    SB();
  };
  auto issue = [&](int j) { issueK(j, 0, 4); issueV(j, 0, 4); STAMP(11 + j); };
  const unsigned E = *(const __attribute__((address_space(4))) unsigned*)pws;
  SB();
  issueV(0, 0, PREV);
  issueK(0, 0, PREK);
  const unsigned tag = E * 0x9E3779B5u + 0x7F4A7C15u;
  const unsigned t8 = tag & 0xffu;
  LAS f32x2* red = (LAS f32x2*)(L + L_RED);
  {
    float v[32];
    rowpair<true>(v, wt[0], wt[1], xq[0].x, xq[1].x, xq[2].x, xq[3].x);
    rowpair<false>(v, wt[2], wt[3], xq[0].y, xq[1].y, xq[2].y, xq[3].y);
    red[w * 64 + lane] = treduce16(v, lane);
    LGKM0();
    const unsigned old = lds_inc(L + L_FLAG + 4, lane);
    CB();
    const f32x2 s = ((red[lane] + red[64 + lane]) + (red[128 + lane] + red[192 + lane])) + ((red[256 + lane] + red[320 + lane]) + (red[384 + lane] + red[448 + lane]));
    const int pr = ch * 4 + (rr4 & 3);
    const unsigned po = isq ? WS_QG + (unsigned)(((kvh * 4 + g) * 256 + r * 16 + pr) * 8) : WS_GU + (unsigned)(((um * 4 + g) * 896 + ucb * 16 + pr) * 8);
    __builtin_amdgcn_raw_buffer_store_b64(v2u{pk_bf16(s.x, s.y), tag}, rws, (old == 7 && r < 30) ? po : OOB, 0, AUX_SC1);
    SB();
    __builtin_amdgcn_s_setprio(0);
    SB();
  }
  STAMP(1);
  if (DIAG == 2) dX = __builtin_amdgcn_s_memrealtime() - dT0;
  issueV(0, PREV, 4);
  issueK(0, PREK, 4);
  STAMP(11);
#pragma unroll
  for (int j = 1; j < QPRE; ++j) issue(j);
  const unsigned qgo = (w < 2 && !EXP_NOQ) ? WS_QG + (unsigned)((kvh * 4 + b_att) * 2048 + w * 1024 + lane * 16) : OOB;
  CB();
  v4u qa = __builtin_amdgcn_raw_buffer_load_b128(rws, qgo, 0, AUX_SC1);
  SB();
  issue(QPRE);
  if (w < 2) {
    if (!EXP_NOQ && __builtin_amdgcn_ballot_w64(qa.y == tag && qa.w == tag) != ~0ull) {
      STAMP(10);
      for (unsigned spin = 0; spin < SPIN_CAP; ++spin) {
        CB();
        qa = __builtin_amdgcn_raw_buffer_load_b128(rws, qgo, 0, AUX_SC1);
        if (__builtin_amdgcn_ballot_w64(qa.y == tag && qa.w == tag) == ~0ull) break;
      }
    }
    *(LAS v2u*)(L + L_Q + (w * 128 + lane * 2) * 4) = v2u{qa.x, qa.z};
    LGKM0();
    (void)lds_inc(L + L_FLAG, lane);
    STAMP(9);
  }
  SB();
#pragma unroll
  for (int j = QPRE + 1; j < NBUF; ++j) issue(j);
  for (unsigned spin = 0; spin < (SPIN_CAP << 4); ++spin) {
    if (*(volatile LAS unsigned*)(L + L_FLAG) >= 2u) break;
    __builtin_amdgcn_s_sleep(1);
  }
  CB();
  STAMP(2);
  if (DIAG == 3) dX = __builtin_amdgcn_s_memrealtime() - dT0;
  v4u qf[4];
#pragma unroll
  for (int i = 0; i < 4; ++i) qf[i] = *(LAS v4u*)(L + L_Q + (((c & 3) * 64 + 16 * i + 4 * g) * 4));

  constexpr float C2 = 0.08838834764831845f * 1.4426950408889634f;
  f32x4 acc[8];
#pragma unroll
  for (int e = 0; e < 8; ++e) acc[e] = f32x4{0.f, 0.f, 0.f, 0.f};
  float m = -INFINITY, l = 0.f;
#pragma unroll
  for (int j = 0; j < 8; ++j) {
    const int bf = j % NBUF;
    f32x4 sc = {0.f, 0.f, 0.f, 0.f};
#pragma unroll
    for (int i = 0; i < 4; ++i) sc = mfma32(kb[bf][i], qf[i], sc);
    const float t0 = sc[0] * C2, t1 = sc[1] * C2, t2 = sc[2] * C2, t3 = sc[3] * C2;
    const float cm = xrow_max(fmaxf(fmaxf(t0, t1), fmaxf(t2, t3)));
    const float mn = fmaxf(m, cm);
    const float alpha = __builtin_amdgcn_exp2f(m - mn);
    const float p0 = __builtin_amdgcn_exp2f(t0 - mn), p1 = __builtin_amdgcn_exp2f(t1 - mn), p2 = __builtin_amdgcn_exp2f(t2 - mn), p3 = __builtin_amdgcn_exp2f(t3 - mn);
    l = l * alpha + ((p0 + p1) + (p2 + p3));
    m = mn;
    const v2u pa = {pk_bf16(p0, p1), pk_bf16(p2, p3)};
    if (j > 0) {
      const float a0 = dpp<0x00>(alpha), a1 = dpp<0x55>(alpha), a2 = dpp<0xAA>(alpha), a3 = dpp<0xFF>(alpha);
#pragma unroll
      for (int e = 0; e < 8; ++e) { acc[e][0] *= a0; acc[e][1] *= a1; acc[e][2] *= a2; acc[e][3] *= a3; }
    }
#pragma unroll
    for (int d = 0; d < 4; ++d) {
      const v2u lo = {__builtin_amdgcn_perm(vb[bf][1][d], vb[bf][0][d], 0x05040100u), __builtin_amdgcn_perm(vb[bf][3][d], vb[bf][2][d], 0x05040100u)};
      const v2u hi = {__builtin_amdgcn_perm(vb[bf][1][d], vb[bf][0][d], 0x07060302u), __builtin_amdgcn_perm(vb[bf][3][d], vb[bf][2][d], 0x07060302u)};
      acc[2 * d] = mfma16(pa, lo, acc[2 * d]);
      acc[2 * d + 1] = mfma16(pa, hi, acc[2 * d + 1]);
    }
    SB();
    if (j + NBUF < 8) issue(j + NBUF);
  }
  STAMP(3);
  l = xrow_sum(l);
  if (g == 0) {
    LAS float* mo = (LAS float*)(L + L_MRG) + w * 512 + 8 * c;
#pragma unroll
    for (int h = 0; h < 4; ++h) {
      *(LAS f32x4*)(mo + h * 128) = f32x4{acc[0][h], acc[1][h], acc[2][h], acc[3][h]};
      *(LAS f32x4*)(mo + h * 128 + 4) = f32x4{acc[4][h], acc[5][h], acc[6][h], acc[7][h]};
    }
    if (c < 4) *(LAS f32x2*)(L + L_ML + (c * 8 + w) * 8) = f32x2{m, l};
  }
  v4u bo[2][4], bd[4];
  if (w < 2) {
    const unsigned colb = (unsigned)(cq * 128 + w * 64 + 4 * c) * 2u;
    v2u r0[8], r1[8], r2[8];
    const unsigned wob = EXP_NOTW ? OOB : (unsigned)(kvh * 512 + rb * 64 + 8 * g) * 1024u + colb;
#pragma unroll
    for (int i = 0; i < 8; ++i) r0[i] = __builtin_amdgcn_raw_buffer_load_b64(rwo, wob + i * 1024, 0, W_AUX);
#pragma unroll
    for (int i = 0; i < 8; ++i) r1[i] = __builtin_amdgcn_raw_buffer_load_b64(rwo, wob + 32768 + i * 1024, 0, W_AUX);
    const unsigned wdb = (unsigned)(pidx * 28 + 8 * g) * 1024u + colb;
#pragma unroll
    for (int i = 0; i < 8; ++i) r2[i] = __builtin_amdgcn_raw_buffer_load_b64(rwd, (8 * g + i < 28 && !EXP_NOTW) ? wdb + i * 1024 : OOB, 0, W_AUX);
    SB();
    tr8x4(r0, bo[0]); tr8x4(r1, bo[1]); tr8x4(r2, bd);
  } else if (w == 2) {
    const unsigned go = c < 14 ? WS_GU + (unsigned)((g * 896 + pidx * 14 + c) * 8) : OOB;
    v4u xv[4];
#pragma unroll
    for (int b = 0; b < 4; ++b) xv[b] = __builtin_amdgcn_raw_buffer_load_b128(rx, lane * 16 + b * 1024, 0, 0);
    const v4u gv = __builtin_amdgcn_raw_buffer_load_b128(rgw, lane * 16, 0, 0);
    v2u ga = {0, 0}, ua = {0, 0};
    for (unsigned spin = 0; spin < SPIN_CAP; ++spin) {
      CB();
      ga = __builtin_amdgcn_raw_buffer_load_b64(rws, go, 0, AUX_SC1);
      ua = __builtin_amdgcn_raw_buffer_load_b64(rws, go + 28672, 0, AUX_SC1);
      if (__builtin_amdgcn_ballot_w64(c >= 14 || (ga.y == tag && ua.y == tag)) == ~0ull) break;
      __builtin_amdgcn_s_sleep(2);
    }
    const float g0 = __uint_as_float(ga.x << 16), g1 = __uint_as_float(ga.x & 0xffff0000u), u0 = __uint_as_float(ua.x << 16), u1 = __uint_as_float(ua.x & 0xffff0000u);
    const float h0 = u0 / (1.f + __expf(-g0)), h1 = u1 / (1.f + __expf(-g1));
    *(LAS unsigned*)(L + L_H + (g * 16 + c) * 4) = c < 14 ? pk_bf16(h0, h1) : 0u;
    float a[4];
#pragma unroll
    for (int b = 0; b < 4; ++b) { a[b] = dot2(xv[b].x, gv.x, 0.f); a[b] = dot2(xv[b].y, gv.y, a[b]); a[b] = dot2(xv[b].z, gv.z, a[b]); a[b] = dot2(xv[b].w, gv.w, a[b]); }
    a[0] = swap32_add(a[0], a[2]); a[1] = swap32_add(a[1], a[3]);
    a[0] = swap16_add(a[0], a[1]);
    a[0] += dpp<ROW_ROR + 8>(a[0]); a[0] += dpp<ROW_ROR + 4>(a[0]); a[0] += dpp<ROW_ROR + 2>(a[0]); a[0] += dpp<ROW_ROR + 1>(a[0]);
    if (c == 0) *(LAS float*)(L + L_GATE + g * 4) = 1.f / (1.f + __expf(-a[0]));
  }
  BAR();
  {
    const int hd = w >> 1, dim = tid & 127;
    f32x4 ml[4]; float ov[8];
#pragma unroll
    for (int i = 0; i < 4; ++i) ml[i] = *(LAS f32x4*)(L + L_ML + hd * 64 + i * 16);
#pragma unroll
    for (int i = 0; i < 8; ++i) ov[i] = ((LAS float*)(L + L_MRG))[i * 512 + hd * 128 + dim];
    const float M = fmaxf(fmaxf(fmaxf(ml[0][0], ml[0][2]), fmaxf(ml[1][0], ml[1][2])), fmaxf(fmaxf(ml[2][0], ml[2][2]), fmaxf(ml[3][0], ml[3][2])));
    float Ls = 0.f, o = 0.f;
#pragma unroll
    for (int i = 0; i < 4; ++i) {
      const float w0 = __builtin_amdgcn_exp2f(ml[i][0] - M), w1 = __builtin_amdgcn_exp2f(ml[i][2] - M);
      Ls += w0 * ml[i][1] + w1 * ml[i][3]; o += w0 * ov[2 * i] + w1 * ov[2 * i + 1];
    }
    o *= __builtin_amdgcn_rcpf(Ls);
    const unsigned lseu = __float_as_uint(M + __builtin_amdgcn_logf(Ls));
    const unsigned nib = (lseu >> ((dim & 7) * 4)) & 15u;
    __builtin_amdgcn_raw_buffer_store_b32(emb(o, (t8 & 0x0fu) | (nib << 4)), rws, WS_PG + (unsigned)(((kvh * 32 + r) * 512 + tid) * 4), 0, AUX_SC1);
    SB();
  }
  STAMP(4);
  if (DIAG == 4) dX = __builtin_amdgcn_s_memrealtime() - dT0;
  const f32x4 zero = {0.f, 0.f, 0.f, 0.f};
  f32x4 db[4];
  float gate = 0.f;
  if (w < 2) {
    const v4u hf = *(LAS v4u*)(L + L_H + ((c & 3) * 32 + 8 * g) * 2);
    gate = *(LAS float*)(L + L_GATE + g * 4);
#pragma unroll
    for (int e = 0; e < 4; ++e) db[e] = mfma32(hf, bd[e], zero);
  }
  {
    const int bb = w >> 1, sp = lane >> 3;
    const unsigned po = WS_PG + (unsigned)(((kvh * 32 + bb * 8 + sp) * 512 + rb * 64 + (w & 1) * 32 + (lane & 7) * 4) * 4);
    v4u pl = {0, 0, 0, 0};
    bool pok = false;
    for (unsigned spin = 0; spin < SPIN_CAP; ++spin) {
      CB();
      const v4u a = __builtin_amdgcn_raw_buffer_load_b128(rws, pok ? OOB : po, 0, AUX_SC1);
      const bool mine = (((a.x ^ t8) | (a.y ^ t8) | (a.z ^ t8) | (a.w ^ t8)) & 0x0fu) == 0u;
      const unsigned long long bm = __builtin_amdgcn_ballot_w64(mine);
      if (!pok && ((bm >> (lane & 56)) & 0xffull) == 0xffull) { pok = true; pl = a; }
      if (__builtin_amdgcn_ballot_w64(!pok) == 0ull) break;
      __builtin_amdgcn_s_sleep(SLP1);
    }
    unsigned half16 = ((pl.x >> 4) & 15u) | (pl.y & 0xf0u) | ((pl.z & 0xf0u) << 4) | ((pl.w & 0xf0u) << 8);
    const unsigned other = __builtin_amdgcn_mov_dpp(half16, 0xB1, 0xf, 0xf, true);
    const float lse = __uint_as_float((lane & 1) ? (other | (half16 << 16)) : (half16 | (other << 16)));
    float M = fmaxf(lse, dpp<ROW_ROR + 8>(lse)); M = xrow_max(M);
    const float wt_ = __builtin_amdgcn_exp2f(lse - M);
    float v0 = wt_ * unemb(pl.x), v1 = wt_ * unemb(pl.y), v2 = wt_ * unemb(pl.z), v3 = wt_ * unemb(pl.w), W = wt_;
    W += dpp<ROW_ROR + 8>(W); W = xrow_sum(W);
    v0 = swap32_add(v0, v2); v1 = swap32_add(v1, v3);
    v0 = swap16_add(v0, v0); v1 = swap16_add(v1, v1);
    v0 += dpp<ROW_ROR + 8>(v0); v1 += dpp<ROW_ROR + 8>(v1);
    const float inv = __builtin_amdgcn_rcpf(W);
    if ((lane & 24) == 0) *(LAS unsigned*)(L + L_O + (bb * 64 + (w & 1) * 32 + (lane & 7) * 4 + (lane >> 5) * 2) * 2) = pk_bf16(v0 * inv, v1 * inv);
  }
  BAR();
  STAMP(5);
  if (DIAG == 5) dX = __builtin_amdgcn_s_memrealtime() - dT0;
  if (w < 2) {
    const v4u of0 = *(LAS v4u*)(L + L_O + ((c & 3) * 64 + 8 * g) * 2), of1 = *(LAS v4u*)(L + L_O + ((c & 3) * 64 + 32 + 8 * g) * 2);
    v4u res;
#pragma unroll
    for (int e = 0; e < 4; ++e) {
      f32x4 da = mfma32(of0, bo[0][e], zero);
      da = mfma32(of1, bo[1][e], da);
      res[e] = emb(gate * sel4(da, g) + (1.f - gate) * sel4(db[e], g), t8);
    }
    const int R = g * 16 + cq * 4 + w * 2 + (c >> 3);
    __builtin_amdgcn_raw_buffer_store_b128(res, rws, WS_FG + (unsigned)(((R * 64 + pidx) * 32 + (c & 7) * 4) * 4), 0, AUX_SC1);
    SB();
  }
  STAMP(6);
  if ((r & 3) == 0) {
    const int R = (r >> 2) * 8 + kvh;
    const unsigned fo = WS_FG + (unsigned)(R * 8192 + w * 1024 + lane * 16);
    v4u pl = {0, 0, 0, 0};
    bool ok = false;
    for (unsigned spin = 0; spin < SPIN_CAP; ++spin) {
      CB();
      const v4u a = __builtin_amdgcn_raw_buffer_load_b128(rws, ok ? OOB : fo, 0, AUX_SC1);
      if (!ok && emb_ok(a, t8)) { ok = true; pl = a; }
      if (__builtin_amdgcn_ballot_w64(!ok) == 0ull) break;
      __builtin_amdgcn_s_sleep(SLP2);
    }
    float v0 = unemb(pl.x), v1 = unemb(pl.y), v2 = unemb(pl.z), v3 = unemb(pl.w);
    v0 = swap32_add(v0, v2); v1 = swap32_add(v1, v3);
    v0 = swap16_add(v0, v0); v1 = swap16_add(v1, v1);
    v0 += dpp<ROW_ROR + 8>(v0); v1 += dpp<ROW_ROR + 8>(v1);
    if ((lane & 24) == 0) *(LAS f32x2*)(L + L_RED + (w * 32 + (lane & 7) * 4 + (lane >> 5) * 2) * 4) = f32x2{v0, v1};
    BAR();
    if (w == 0 && lane < 16) {
      const LAS f32x2* fr = (const LAS f32x2*)(L + L_RED) + lane;
      const f32x2 s = ((fr[0] + fr[16]) + (fr[32] + fr[48])) + ((fr[64] + fr[80]) + (fr[96] + fr[112]));
      ((unsigned*)pout)[R * 16 + lane] = pk_bf16(s.x, s.y);
      if (blk == 0 && lane == 0) __hip_atomic_store((unsigned*)pws, tag, __ATOMIC_RELAXED, __HIP_MEMORY_SCOPE_AGENT);
    }
  }
  STAMP(7);
  if (DIAG && blk == 0 && w == 0) {
    const unsigned long long te = __builtin_amdgcn_s_memrealtime();
    if (DIAG == 1) dX = te - dT0;
    while (__builtin_amdgcn_s_memrealtime() - te < dX * DMUL) __builtin_amdgcn_s_sleep(4);
  }
}
}

extern "C" void kernel_launch(void* const* d_in, const int* in_sizes, int n_in, void* d_out, int out_size, void* d_ws,
                              size_t ws_size, hipStream_t stream) {
  fk::fused_kernel<<<256, 512, 0, stream>>>(d_in[0], d_in[1], d_in[2], d_in[3], d_in[4], d_in[6], d_in[7], d_in[8], d_in[9], d_out, d_ws);
}
```

```cpp
#include <hip/hip_runtime.h>
#include <cstdint>

#define K_AUX 2
#define V_AUX 2
#define NBUF 5
#define STAMPS 0
#define W_AUX 2
#define QPRE 4
#define PREK 4
#define PREV 4
#define EXP_NOTILE 0
#define EXP_NOTW 0
#define EXP_NOX 0
#define EXP_NOQ 0
#define X_AUX 0
#define SLP1 2
#define SLP2 2
#define SPEC 2

typedef unsigned v4u __attribute__((ext_vector_type(4)));
typedef unsigned v2u __attribute__((ext_vector_type(2)));
typedef float f32x4 __attribute__((ext_vector_type(4)));
typedef float f32x2 __attribute__((ext_vector_type(2)));
typedef short s16x4 __attribute__((ext_vector_type(4)));
typedef __bf16 bf16x8 __attribute__((ext_vector_type(8)));
typedef __bf16 bf16x2 __attribute__((ext_vector_type(2)));
typedef __amdgpu_buffer_rsrc_t rsrc_t;
#define LAS __attribute__((address_space(3)))

namespace fk {
constexpr int AUX_NT = 2, AUX_SC1 = 16;
constexpr unsigned OOB = 0x40000000u;
constexpr unsigned WS_QG = 4096;
constexpr unsigned WS_GU = WS_QG + 65536;
constexpr unsigned WS_ST = WS_GU + 57344;
constexpr unsigned WS_PG = WS_ST + 8192;
constexpr unsigned WS_FG = WS_PG + 524288;
constexpr unsigned WS_END = WS_FG + 524288;
constexpr unsigned WS_STAMP = 8u << 20;
constexpr unsigned SPIN_CAP = 1u << 17;

constexpr int L_RED = 0;
constexpr int L_Q = 4096;
constexpr int L_H = L_Q + 1024;
constexpr int L_GATE = L_H + 256;
constexpr int L_FLAG = L_GATE + 16;
constexpr int L_O = L_FLAG + 16;
constexpr int L_ML = L_O + 512;
constexpr int L_MRG = L_ML + 256;
constexpr int L_END = L_MRG + 16384;

__device__ __forceinline__ rsrc_t mk(const void* p, unsigned bytes) {
  return __builtin_amdgcn_make_buffer_rsrc((void*)p, 0, (int)bytes, 0x00020000);
}
__device__ __forceinline__ float dot2(unsigned a, unsigned b, float c) {
  return __builtin_amdgcn_fdot2_f32_bf16(__builtin_bit_cast(bf16x2, a), __builtin_bit_cast(bf16x2, b), c, false);
}
__device__ __forceinline__ unsigned pk_bf16(float lo, float hi) {
  f32x2 v = {lo, hi};
  return __builtin_bit_cast(unsigned, __builtin_convertvector(v, bf16x2));
}
template <int CTRL> __device__ __forceinline__ float dpp(float x) {
  return __builtin_bit_cast(float, __builtin_amdgcn_mov_dpp(__builtin_bit_cast(int, x), CTRL, 0xf, 0xf, true));
}
__device__ __forceinline__ float swap32_add(float a, float b) {
  auto s = __builtin_amdgcn_permlane32_swap(__float_as_uint(a), __float_as_uint(b), false, false);
  return __uint_as_float(s[0]) + __uint_as_float(s[1]);
}
__device__ __forceinline__ float swap16_add(float a, float b) {
  auto s = __builtin_amdgcn_permlane16_swap(__float_as_uint(a), __float_as_uint(b), false, false);
  return __uint_as_float(s[0]) + __uint_as_float(s[1]);
}
__device__ __forceinline__ float xrow_max(float x) {
  auto s = __builtin_amdgcn_permlane16_swap(__float_as_uint(x), __float_as_uint(x), false, false);
  x = fmaxf(__uint_as_float(s[0]), __uint_as_float(s[1]));
  auto t = __builtin_amdgcn_permlane32_swap(__float_as_uint(x), __float_as_uint(x), false, false);
  return fmaxf(__uint_as_float(t[0]), __uint_as_float(t[1]));
}
__device__ __forceinline__ float xrow_sum(float x) {
  x = swap16_add(x, x);
  return swap32_add(x, x);
}
constexpr int ROW_SHL = 0x100, ROW_SHR = 0x110, ROW_ROR = 0x120;
__device__ __forceinline__ f32x2 treduce16(float (&v)[32], int lane) {
#pragma unroll
  for (int i = 0; i < 16; ++i) v[i] = swap32_add(v[i], v[i + 16]);
#pragma unroll
  for (int i = 0; i < 8; ++i) v[i] = swap16_add(v[i], v[i + 8]);
#pragma unroll
  for (int i = 0; i < 4; ++i) { float t1 = v[i] + dpp<ROW_ROR + 8>(v[i]), t2 = v[i + 4] + dpp<ROW_ROR + 8>(v[i + 4]); v[i] = (lane & 8) ? t2 : t1; }
#pragma unroll
  for (int i = 0; i < 2; ++i) { float t1 = v[i] + dpp<ROW_SHL + 4>(v[i]), t2 = v[i + 2] + dpp<ROW_SHR + 4>(v[i + 2]); v[i] = (lane & 4) ? t2 : t1; }
  return f32x2{v[0], v[1]};
}
template <bool FIRST>
__device__ __forceinline__ void rowpair(float (&v)[32], const v4u& w0, const v4u& w1, unsigned x0, unsigned x1, unsigned x2, unsigned x3) {
  const unsigned xp[4] = {x0, x1, x2, x3};
#pragma unroll
  for (int d = 0; d < 4; ++d) {
    const unsigned lo = __builtin_amdgcn_perm(w1[d], w0[d], 0x05040100u), hi = __builtin_amdgcn_perm(w1[d], w0[d], 0x07060302u);
#pragma unroll
    for (int b = 0; b < 4; ++b) {
      v[b * 8 + 2 * d] = dot2(lo, xp[b], FIRST ? 0.f : v[b * 8 + 2 * d]);
      v[b * 8 + 2 * d + 1] = dot2(hi, xp[b], FIRST ? 0.f : v[b * 8 + 2 * d + 1]);
    }
  }
}
__device__ __forceinline__ f32x4 mfma32(v4u a, v4u b, f32x4 c) {
  return __builtin_amdgcn_mfma_f32_16x16x32_bf16(__builtin_bit_cast(bf16x8, a), __builtin_bit_cast(bf16x8, b), c, 0, 0, 0);
}
__device__ __forceinline__ f32x4 mfma16(v2u a, v2u b, f32x4 c) {
  return __builtin_amdgcn_mfma_f32_16x16x16bf16_1k(__builtin_bit_cast(s16x4, a), __builtin_bit_cast(s16x4, b), c, 0, 0, 0);
}
__device__ __forceinline__ float sel4(const f32x4& d, int g) { return g == 0 ? d[0] : g == 1 ? d[1] : g == 2 ? d[2] : d[3]; }
__device__ __forceinline__ unsigned lds_inc(LAS unsigned char* p, int lane) {
  unsigned old = 0;
  if (lane == 0) old = __hip_atomic_fetch_add((LAS unsigned*)p, 1u, __ATOMIC_RELAXED, __HIP_MEMORY_SCOPE_WORKGROUP);
  return __builtin_amdgcn_readfirstlane(old);
}
__device__ __forceinline__ unsigned emb(float v, unsigned t8) { return ((__float_as_uint(v) + 0x80u) & 0xffffff00u) | t8; }
__device__ __forceinline__ bool emb_ok(const v4u& a, unsigned t8) { return (((a.x ^ t8) | (a.y ^ t8) | (a.z ^ t8) | (a.w ^ t8)) & 0xffu) == 0u; }
__device__ __forceinline__ float unemb(unsigned u) { return __uint_as_float(u & 0xffffff00u); }
__device__ __forceinline__ void tr8x4(const v2u (&r)[8], v4u (&f)[4]) {
#pragma unroll
  for (int i = 0; i < 4; ++i) {
    f[0][i] = __builtin_amdgcn_perm(r[2 * i + 1].x, r[2 * i].x, 0x05040100u); f[1][i] = __builtin_amdgcn_perm(r[2 * i + 1].x, r[2 * i].x, 0x07060302u);
    f[2][i] = __builtin_amdgcn_perm(r[2 * i + 1].y, r[2 * i].y, 0x05040100u); f[3][i] = __builtin_amdgcn_perm(r[2 * i + 1].y, r[2 * i].y, 0x07060302u);
  }
}
#define SB() __builtin_amdgcn_sched_barrier(0)
#define CB() asm volatile("" ::: "memory")
#define LGKM0() asm volatile("s_waitcnt lgkmcnt(0)" ::: "memory")
#define BAR() do { LGKM0(); __builtin_amdgcn_s_barrier(); CB(); } while (0)
#define STAMP(i) do { } while (0)

__global__ void __launch_bounds__(512, 2)
fused_kernel(const void* __restrict__ px, const void* __restrict__ pk, const void* __restrict__ pv, const void* __restrict__ pwq,
             const void* __restrict__ pwo, const void* __restrict__ pwg, const void* __restrict__ pwu, const void* __restrict__ pwd,
             const void* __restrict__ pgw, void* __restrict__ pout, void* pws) {
  __shared__ __attribute__((aligned(16))) unsigned char smem[L_END];
  LAS unsigned char* L = (LAS unsigned char*)smem;
  STAMP(0);
  asm volatile("" :: "s"(px), "s"(pk), "s"(pv), "s"(pwq), "s"(pwo), "s"(pwg), "s"(pwu), "s"(pwd), "s"(pgw), "s"(pout), "s"(pws));
  const int tid = threadIdx.x, lane = tid & 63, w = __builtin_amdgcn_readfirstlane(tid >> 6);
  const int g = lane >> 4, c = lane & 15;
  const int blk = blockIdx.x, kvh = blk & 7, r = blk >> 3;
  const int b_att = r >> 3, s_att = r & 7;
  const int rb = r >> 2, cq = r & 3;
  const int pidx = kvh * 8 + rb;

  const size_t kvoff = ((size_t)(b_att * 8 + kvh) * 8192 + s_att * 1024) * 256;
  const rsrc_t rk = mk((const char*)pk + kvoff, 262144), rv = mk((const char*)pv + kvoff, 262144);
  const rsrc_t rws = mk(pws, WS_END);
  const rsrc_t rx = mk(px, 4096), rgw = mk(pgw, 1024);
  const rsrc_t rwo = mk(pwo, 4096 * 512 * 2), rwd = mk(pwd, 1792 * 512 * 2);

  __builtin_amdgcn_s_setprio(3);
  const int rr4 = lane >> 2, ch = lane & 3;
  const bool isq = r < 16;
  const int um = (r - 16) >= 7 ? 1 : 0;
  const int ucb = kvh * 7 + (r - 16) - 7 * um;
  v4u wt[4]; v2u xq[4];
  {
    const int row = w * 64 + 4 * rr4;
    const unsigned rstr = isq ? 8192u : 3584u;
    const rsrc_t ru = mk(isq ? pwq : (um ? pwu : pwg), isq ? 512 * 4096 * 2 : 512 * 1792 * 2);
    const unsigned uo = (r < 30 && !EXP_NOTILE) ? (unsigned)row * rstr + (unsigned)((isq ? kvh * 16 + r : ucb) * 32 + ch * 8) * 2u : OOB;
#pragma unroll
    for (int i = 0; i < 4; ++i) wt[i] = __builtin_amdgcn_raw_buffer_load_b128(ru, uo + i * rstr, 0, W_AUX);
#pragma unroll
    for (int b = 0; b < 4; ++b) xq[b] = __builtin_amdgcn_raw_buffer_load_b64(rx, EXP_NOX ? OOB : row * 2 + b * 1024, 0, X_AUX);
  }
  SB();
  if (tid == 0) { *(volatile LAS v4u*)(L + L_FLAG) = v4u{0u, 0u, 0u, 0u}; }
  BAR();
  SB();
  const unsigned ko = c * 256 + g * 16, vo = g * 1024 + c * 16;
  v4u kb[NBUF][4], vb[NBUF][4];
  auto issueK = [&](int j, int i0, int i1) {
    const unsigned so = (unsigned)(j * 8 + w) * 4096u;
#pragma unroll
    for (int i = i0; i < i1; ++i) kb[j % NBUF][i] = __builtin_amdgcn_raw_buffer_load_b128(rk, ko + i * 64, so, K_AUX);
    SB();
  };
  auto issueV = [&](int j, int i0, int i1) {
    const unsigned so = (unsigned)(j * 8 + w) * 4096u;
#pragma unroll
    for (int i = i0; i < i1; ++i) vb[j % NBUF][i] = __builtin_amdgcn_raw_buffer_load_b128(rv, vo + i * 256, so, V_AUX);
    SB();
  };
  auto issue = [&](int j) { issueK(j, 0, 4); issueV(j, 0, 4); STAMP(11 + j); };
  const unsigned E = *(const __attribute__((address_space(4))) unsigned*)pws;
  SB();
  issueV(0, 0, PREV);
  issueK(0, 0, PREK);
  const unsigned tag = E * 0x9E3779B5u + 0x7F4A7C15u;
  const unsigned t8 = tag & 0xffu;
  LAS f32x2* red = (LAS f32x2*)(L + L_RED);
  {
    float v[32];
    rowpair<true>(v, wt[0], wt[1], xq[0].x, xq[1].x, xq[2].x, xq[3].x);
    rowpair<false>(v, wt[2], wt[3], xq[0].y, xq[1].y, xq[2].y, xq[3].y);
    red[w * 64 + lane] = treduce16(v, lane);
    LGKM0();
    const unsigned old = lds_inc(L + L_FLAG + 4, lane);
    CB();
    const f32x2 s = ((red[lane] + red[64 + lane]) + (red[128 + lane] + red[192 + lane])) + ((red[256 + lane] + red[320 + lane]) + (red[384 + lane] + red[448 + lane]));
    const int pr = ch * 4 + (rr4 & 3);
    const unsigned po = isq ? WS_QG + (unsigned)(((kvh * 4 + g) * 256 + r * 16 + pr) * 8) : WS_GU + (unsigned)(((um * 4 + g) * 896 + ucb * 16 + pr) * 8);
    __builtin_amdgcn_raw_buffer_store_b64(v2u{pk_bf16(s.x, s.y), tag}, rws, (old == 7 && r < 30) ? po : OOB, 0, AUX_SC1);
    SB();
    __builtin_amdgcn_s_setprio(0);
    SB();
  }
  STAMP(1);
  issueV(0, PREV, 4);
  issueK(0, PREK, 4);
  STAMP(11);
#pragma unroll
  for (int j = 1; j < QPRE; ++j) issue(j);
  const unsigned qgo = (w < 2 && !EXP_NOQ) ? WS_QG + (unsigned)((kvh * 4 + b_att) * 2048 + w * 1024 + lane * 16) : OOB;
  CB();
  v4u qa = __builtin_amdgcn_raw_buffer_load_b128(rws, qgo, 0, AUX_SC1);
  SB();
  issue(QPRE);
  if (w < 2) {
    if (!EXP_NOQ && __builtin_amdgcn_ballot_w64(qa.y == tag && qa.w == tag) != ~0ull) {
      STAMP(10);
      for (unsigned spin = 0; spin < SPIN_CAP; ++spin) {
        CB();
        qa = __builtin_amdgcn_raw_buffer_load_b128(rws, qgo, 0, AUX_SC1);
        if (__builtin_amdgcn_ballot_w64(qa.y == tag && qa.w == tag) == ~0ull) break;
      }
    }
    *(LAS v2u*)(L + L_Q + (w * 128 + lane * 2) * 4) = v2u{qa.x, qa.z};
    LGKM0();
    (void)lds_inc(L + L_FLAG, lane);
    STAMP(9);
  }
  SB();
#pragma unroll
  for (int j = QPRE + 1; j < NBUF; ++j) issue(j);
  for (unsigned spin = 0; spin < (SPIN_CAP << 4); ++spin) {
    if (*(volatile LAS unsigned*)(L + L_FLAG) >= 2u) break;
    __builtin_amdgcn_s_sleep(1);
  }
  CB();
  STAMP(2);
  v4u qf[4];
#pragma unroll
  for (int i = 0; i < 4; ++i) qf[i] = *(LAS v4u*)(L + L_Q + (((c & 3) * 64 + 16 * i + 4 * g) * 4));

  constexpr float C2 = 0.08838834764831845f * 1.4426950408889634f;
  f32x4 acc[8];
#pragma unroll
  for (int e = 0; e < 8; ++e) acc[e] = f32x4{0.f, 0.f, 0.f, 0.f};
  float m = -INFINITY, l = 0.f;
#pragma unroll
  for (int j = 0; j < 8; ++j) {
    const int bf = j % NBUF;
    f32x4 sc = {0.f, 0.f, 0.f, 0.f};
#pragma unroll
    for (int i = 0; i < 4; ++i) sc = mfma32(kb[bf][i], qf[i], sc);
    const float t0 = sc[0] * C2, t1 = sc[1] * C2, t2 = sc[2] * C2, t3 = sc[3] * C2;
    const float cm = xrow_max(fmaxf(fmaxf(t0, t1), fmaxf(t2, t3)));
    const float mn = fmaxf(m, cm);
    const float alpha = __builtin_amdgcn_exp2f(m - mn);
    const float p0 = __builtin_amdgcn_exp2f(t0 - mn), p1 = __builtin_amdgcn_exp2f(t1 - mn), p2 = __builtin_amdgcn_exp2f(t2 - mn), p3 = __builtin_amdgcn_exp2f(t3 - mn);
    l = l * alpha + ((p0 + p1) + (p2 + p3));
    m = mn;
    const v2u pa = {pk_bf16(p0, p1), pk_bf16(p2, p3)};
    if (j > 0) {
      const float a0 = dpp<0x00>(alpha), a1 = dpp<0x55>(alpha), a2 = dpp<0xAA>(alpha), a3 = dpp<0xFF>(alpha);
#pragma unroll
      for (int e = 0; e < 8; ++e) { acc[e][0] *= a0; acc[e][1] *= a1; acc[e][2] *= a2; acc[e][3] *= a3; }
    }
#pragma unroll
    for (int d = 0; d < 4; ++d) {
      const v2u lo = {__builtin_amdgcn_perm(vb[bf][1][d], vb[bf][0][d], 0x05040100u), __builtin_amdgcn_perm(vb[bf][3][d], vb[bf][2][d], 0x05040100u)};
      const v2u hi = {__builtin_amdgcn_perm(vb[bf][1][d], vb[bf][0][d], 0x07060302u), __builtin_amdgcn_perm(vb[bf][3][d], vb[bf][2][d], 0x07060302u)};
      acc[2 * d] = mfma16(pa, lo, acc[2 * d]);
      acc[2 * d + 1] = mfma16(pa, hi, acc[2 * d + 1]);
    }
    SB();
    if (j + NBUF < 8) issue(j + NBUF);
  }
  STAMP(3);
  l = xrow_sum(l);
  if (g == 0) {
    LAS float* mo = (LAS float*)(L + L_MRG) + w * 512 + 8 * c;
#pragma unroll
    for (int h = 0; h < 4; ++h) {
      *(LAS f32x4*)(mo + h * 128) = f32x4{acc[0][h], acc[1][h], acc[2][h], acc[3][h]};
      *(LAS f32x4*)(mo + h * 128 + 4) = f32x4{acc[4][h], acc[5][h], acc[6][h], acc[7][h]};
    }
    if (c < 4) *(LAS f32x2*)(L + L_ML + (c * 8 + w) * 8) = f32x2{m, l};
  }
  v4u bo[2][4], bd[4];
  if (w < 2) {
    const unsigned colb = (unsigned)(cq * 128 + w * 64 + 4 * c) * 2u;
    v2u r0[8], r1[8], r2[8];
    const unsigned wob = EXP_NOTW ? OOB : (unsigned)(kvh * 512 + rb * 64 + 8 * g) * 1024u + colb;
#pragma unroll
    for (int i = 0; i < 8; ++i) r0[i] = __builtin_amdgcn_raw_buffer_load_b64(rwo, wob + i * 1024, 0, W_AUX);
#pragma unroll
    for (int i = 0; i < 8; ++i) r1[i] = __builtin_amdgcn_raw_buffer_load_b64(rwo, wob + 32768 + i * 1024, 0, W_AUX);
    const unsigned wdb = (unsigned)(pidx * 28 + 8 * g) * 1024u + colb;
#pragma unroll
    for (int i = 0; i < 8; ++i) r2[i] = __builtin_amdgcn_raw_buffer_load_b64(rwd, (8 * g + i < 28 && !EXP_NOTW) ? wdb + i * 1024 : OOB, 0, W_AUX);
    SB();
    tr8x4(r0, bo[0]); tr8x4(r1, bo[1]); tr8x4(r2, bd);
  } else if (w == 2) {
    const unsigned go = c < 14 ? WS_GU + (unsigned)((g * 896 + pidx * 14 + c) * 8) : OOB;
    v4u xv[4];
#pragma unroll
    for (int b = 0; b < 4; ++b) xv[b] = __builtin_amdgcn_raw_buffer_load_b128(rx, lane * 16 + b * 1024, 0, 0);
    const v4u gv = __builtin_amdgcn_raw_buffer_load_b128(rgw, lane * 16, 0, 0);
    v2u ga = {0, 0}, ua = {0, 0};
    for (unsigned spin = 0; spin < SPIN_CAP; ++spin) {
      CB();
      ga = __builtin_amdgcn_raw_buffer_load_b64(rws, go, 0, AUX_SC1);
      ua = __builtin_amdgcn_raw_buffer_load_b64(rws, go + 28672, 0, AUX_SC1);
      if (__builtin_amdgcn_ballot_w64(c >= 14 || (ga.y == tag && ua.y == tag)) == ~0ull) break;
      __builtin_amdgcn_s_sleep(2);
    }
    const float g0 = __uint_as_float(ga.x << 16), g1 = __uint_as_float(ga.x & 0xffff0000u), u0 = __uint_as_float(ua.x << 16), u1 = __uint_as_float(ua.x & 0xffff0000u);
    const float h0 = u0 / (1.f + __expf(-g0)), h1 = u1 / (1.f + __expf(-g1));
    *(LAS unsigned*)(L + L_H + (g * 16 + c) * 4) = c < 14 ? pk_bf16(h0, h1) : 0u;
    float a[4];
#pragma unroll
    for (int b = 0; b < 4; ++b) { a[b] = dot2(xv[b].x, gv.x, 0.f); a[b] = dot2(xv[b].y, gv.y, a[b]); a[b] = dot2(xv[b].z, gv.z, a[b]); a[b] = dot2(xv[b].w, gv.w, a[b]); }
    a[0] = swap32_add(a[0], a[2]); a[1] = swap32_add(a[1], a[3]);
    a[0] = swap16_add(a[0], a[1]);
    a[0] += dpp<ROW_ROR + 8>(a[0]); a[0] += dpp<ROW_ROR + 4>(a[0]); a[0] += dpp<ROW_ROR + 2>(a[0]); a[0] += dpp<ROW_ROR + 1>(a[0]);
    if (c == 0) *(LAS float*)(L + L_GATE + g * 4) = 1.f / (1.f + __expf(-a[0]));
  }
  BAR();
  {
    const int hd = w >> 1, dim = tid & 127;
    f32x4 ml[4]; float ov[8];
#pragma unroll
    for (int i = 0; i < 4; ++i) ml[i] = *(LAS f32x4*)(L + L_ML + hd * 64 + i * 16);
#pragma unroll
    for (int i = 0; i < 8; ++i) ov[i] = ((LAS float*)(L + L_MRG))[i * 512 + hd * 128 + dim];
    const float M = fmaxf(fmaxf(fmaxf(ml[0][0], ml[0][2]), fmaxf(ml[1][0], ml[1][2])), fmaxf(fmaxf(ml[2][0], ml[2][2]), fmaxf(ml[3][0], ml[3][2])));
    float Ls = 0.f, o = 0.f;
#pragma unroll
    for (int i = 0; i < 4; ++i) {
      const float w0 = __builtin_amdgcn_exp2f(ml[i][0] - M), w1 = __builtin_amdgcn_exp2f(ml[i][2] - M);
      Ls += w0 * ml[i][1] + w1 * ml[i][3]; o += w0 * ov[2 * i] + w1 * ov[2 * i + 1];
    }
    o *= __builtin_amdgcn_rcpf(Ls);
    const unsigned lseu = __float_as_uint(M + __builtin_amdgcn_logf(Ls));
    const unsigned nib = (lseu >> ((dim & 7) * 4)) & 15u;
    __builtin_amdgcn_raw_buffer_store_b32(emb(o, (t8 & 0x0fu) | (nib << 4)), rws, WS_PG + (unsigned)(((kvh * 32 + r) * 512 + tid) * 4), 0, AUX_SC1);
    SB();
  }
  STAMP(4);
  const f32x4 zero = {0.f, 0.f, 0.f, 0.f};
  f32x4 db[4];
  float gate = 0.f;
  if (w < 2) {
    const v4u hf = *(LAS v4u*)(L + L_H + ((c & 3) * 32 + 8 * g) * 2);
    gate = *(LAS float*)(L + L_GATE + g * 4);
#pragma unroll
    for (int e = 0; e < 4; ++e) db[e] = mfma32(hf, bd[e], zero);
  }
  {
    const int bb = w >> 1, sp = lane >> 3;
    const unsigned po = WS_PG + (unsigned)(((kvh * 32 + bb * 8 + sp) * 512 + rb * 64 + (w & 1) * 32 + (lane & 7) * 4) * 4);
    v4u pl = {0, 0, 0, 0};
    bool pok = false;
    for (unsigned spin = 0; spin < SPIN_CAP; ++spin) {
      CB();
      const v4u a = __builtin_amdgcn_raw_buffer_load_b128(rws, pok ? OOB : po, 0, AUX_SC1);
      const bool mine = (((a.x ^ t8) | (a.y ^ t8) | (a.z ^ t8) | (a.w ^ t8)) & 0x0fu) == 0u;
      const unsigned long long bm = __builtin_amdgcn_ballot_w64(mine);
      if (!pok && ((bm >> (lane & 56)) & 0xffull) == 0xffull) { pok = true; pl = a; }
      if (__builtin_amdgcn_ballot_w64(!pok) == 0ull) break;
      __builtin_amdgcn_s_sleep(SLP1);
    }
    unsigned half16 = ((pl.x >> 4) & 15u) | (pl.y & 0xf0u) | ((pl.z & 0xf0u) << 4) | ((pl.w & 0xf0u) << 8);
    const unsigned other = __builtin_amdgcn_mov_dpp(half16, 0xB1, 0xf, 0xf, true);
    const float lse = __uint_as_float((lane & 1) ? (other | (half16 << 16)) : (half16 | (other << 16)));
    float M = fmaxf(lse, dpp<ROW_ROR + 8>(lse)); M = xrow_max(M);
    const float wt_ = __builtin_amdgcn_exp2f(lse - M);
    float v0 = wt_ * unemb(pl.x), v1 = wt_ * unemb(pl.y), v2 = wt_ * unemb(pl.z), v3 = wt_ * unemb(pl.w), W = wt_;
    W += dpp<ROW_ROR + 8>(W); W = xrow_sum(W);
    v0 = swap32_add(v0, v2); v1 = swap32_add(v1, v3);
    v0 = swap16_add(v0, v0); v1 = swap16_add(v1, v1);
    v0 += dpp<ROW_ROR + 8>(v0); v1 += dpp<ROW_ROR + 8>(v1);
    const float inv = __builtin_amdgcn_rcpf(W);
    if ((lane & 24) == 0) *(LAS unsigned*)(L + L_O + (bb * 64 + (w & 1) * 32 + (lane & 7) * 4 + (lane >> 5) * 2) * 2) = pk_bf16(v0 * inv, v1 * inv);
  }
  BAR();
  STAMP(5);
  if (w < 2) {
    const v4u of0 = *(LAS v4u*)(L + L_O + ((c & 3) * 64 + 8 * g) * 2), of1 = *(LAS v4u*)(L + L_O + ((c & 3) * 64 + 32 + 8 * g) * 2);
    v4u res;
#pragma unroll
    for (int e = 0; e < 4; ++e) {
      f32x4 da = mfma32(of0, bo[0][e], zero);
      da = mfma32(of1, bo[1][e], da);
      res[e] = emb(gate * sel4(da, g) + (1.f - gate) * sel4(db[e], g), t8);
    }
    const int R = g * 16 + cq * 4 + w * 2 + (c >> 3);
    __builtin_amdgcn_raw_buffer_store_b128(res, rws, WS_FG + (unsigned)(((R * 64 + pidx) * 32 + (c & 7) * 4) * 4), 0, AUX_SC1);
    SB();
  }
  STAMP(6);
  if ((r & 3) == 0) {
    const int R = (r >> 2) * 8 + kvh;
    const unsigned fo = WS_FG + (unsigned)(R * 8192 + w * 1024 + lane * 16);
    v4u pl = {0, 0, 0, 0};
    bool ok = false;
    for (unsigned spin = 0; spin < SPIN_CAP; ++spin) {
      CB();
      const v4u a = __builtin_amdgcn_raw_buffer_load_b128(rws, ok ? OOB : fo, 0, AUX_SC1);
      if (!ok && emb_ok(a, t8)) { ok = true; pl = a; }
      if (__builtin_amdgcn_ballot_w64(!ok) == 0ull) break;
      __builtin_amdgcn_s_sleep(SLP2);
    }
    float v0 = unemb(pl.x), v1 = unemb(pl.y), v2 = unemb(pl.z), v3 = unemb(pl.w);
    v0 = swap32_add(v0, v2); v1 = swap32_add(v1, v3);
    v0 = swap16_add(v0, v0); v1 = swap16_add(v1, v1);
    v0 += dpp<ROW_ROR + 8>(v0); v1 += dpp<ROW_ROR + 8>(v1);
    if ((lane & 24) == 0) *(LAS f32x2*)(L + L_RED + (w * 32 + (lane & 7) * 4 + (lane >> 5) * 2) * 4) = f32x2{v0, v1};
    BAR();
    if (w == 0 && lane < 16) {
      const LAS f32x2* fr = (const LAS f32x2*)(L + L_RED) + lane;
      const f32x2 s = ((fr[0] + fr[16]) + (fr[32] + fr[48])) + ((fr[64] + fr[80]) + (fr[96] + fr[112]));
      ((unsigned*)pout)[R * 16 + lane] = pk_bf16(s.x, s.y);
      if (blk == 0 && lane == 0) __hip_atomic_store((unsigned*)pws, tag, __ATOMIC_RELAXED, __HIP_MEMORY_SCOPE_AGENT);
    }
  }
  STAMP(7);
}
}

extern "C" void kernel_launch(void* const* d_in, const int* in_sizes, int n_in, void* d_out, int out_size, void* d_ws,
                              size_t ws_size, hipStream_t stream) {
  fk::fused_kernel<<<256, 512, 0, stream>>>(d_in[0], d_in[1], d_in[2], d_in[3], d_in[4], d_in[6], d_in[7], d_in[8], d_in[9], d_out, d_ws);
}
```

```cpp
#define SLP1 0
#define SLP2 0
#define WSLP 1
#define W_AUX 0
#include <hip/hip_runtime.h>
#include <cstdint>

#define K_AUX 2
#define V_AUX 2
#define DEFER 1
#define PIPE 1
#define GAP1 0
#define GAP2 0
#define DTH 8u
#define EXP_HALF 0
#define NBUF 5
#define STAMPS 0
#define QPRE 4
#define PREK 4
#define PREV 4
#define EXP_NOTILE 0
#define EXP_NOTW 0
#define EXP_NOX 0
#define EXP_NOQ 0
#define X_AUX 0
#define SPEC 2

typedef unsigned v4u __attribute__((ext_vector_type(4)));
typedef unsigned v2u __attribute__((ext_vector_type(2)));
typedef float f32x4 __attribute__((ext_vector_type(4)));
typedef float f32x2 __attribute__((ext_vector_type(2)));
typedef short s16x4 __attribute__((ext_vector_type(4)));
typedef __bf16 bf16x8 __attribute__((ext_vector_type(8)));
typedef __bf16 bf16x2 __attribute__((ext_vector_type(2)));
typedef __amdgpu_buffer_rsrc_t rsrc_t;
#define LAS __attribute__((address_space(3)))

namespace fk {
constexpr int AUX_NT = 2, AUX_SC1 = 16;
constexpr unsigned OOB = 0x40000000u;
constexpr unsigned WS_QG = 4096;
constexpr unsigned WS_GU = WS_QG + 131072;
constexpr unsigned WS_PG = WS_GU + 114688;
constexpr unsigned WS_FG = WS_PG + 524288;
constexpr unsigned WS_END = WS_FG + 524288;
constexpr unsigned WS_STAMP = 8u << 20;
constexpr unsigned SPIN_CAP = 1u << 17;

constexpr int L_RED = 0;
constexpr int L_Q = 8192;
constexpr int L_H = L_Q + 1024;
constexpr int L_GATE = L_H + 256;
constexpr int L_FLAG = L_GATE + 16;
constexpr int L_FLAG2 = L_FLAG + 16;
constexpr int L_O = L_FLAG2 + 16;
constexpr int L_ML = L_O + 512;
constexpr int L_MRG = L_ML + 256;
constexpr int L_END = L_MRG + 16384;

__device__ __forceinline__ rsrc_t mk(const void* p, unsigned bytes) {
  return __builtin_amdgcn_make_buffer_rsrc((void*)p, 0, (int)bytes, 0x00020000);
}
__device__ __forceinline__ float dot2(unsigned a, unsigned b, float c) {
  return __builtin_amdgcn_fdot2_f32_bf16(__builtin_bit_cast(bf16x2, a), __builtin_bit_cast(bf16x2, b), c, false);
}
__device__ __forceinline__ unsigned pk_bf16(float lo, float hi) {
  f32x2 v = {lo, hi};
  return __builtin_bit_cast(unsigned, __builtin_convertvector(v, bf16x2));
}
template <int CTRL> __device__ __forceinline__ float dpp(float x) {
  return __builtin_bit_cast(float, __builtin_amdgcn_mov_dpp(__builtin_bit_cast(int, x), CTRL, 0xf, 0xf, true));
}
__device__ __forceinline__ float swap32_add(float a, float b) {
  auto s = __builtin_amdgcn_permlane32_swap(__float_as_uint(a), __float_as_uint(b), false, false);
  return __uint_as_float(s[0]) + __uint_as_float(s[1]);
}
__device__ __forceinline__ float swap16_add(float a, float b) {
  auto s = __builtin_amdgcn_permlane16_swap(__float_as_uint(a), __float_as_uint(b), false, false);
  return __uint_as_float(s[0]) + __uint_as_float(s[1]);
}
__device__ __forceinline__ float xrow_max(float x) {
  auto s = __builtin_amdgcn_permlane16_swap(__float_as_uint(x), __float_as_uint(x), false, false);
  x = fmaxf(__uint_as_float(s[0]), __uint_as_float(s[1]));
  auto t = __builtin_amdgcn_permlane32_swap(__float_as_uint(x), __float_as_uint(x), false, false);
  return fmaxf(__uint_as_float(t[0]), __uint_as_float(t[1]));
}
__device__ __forceinline__ float xrow_sum(float x) {
  x = swap16_add(x, x);
  return swap32_add(x, x);
}
constexpr int ROW_SHL = 0x100, ROW_SHR = 0x110, ROW_ROR = 0x120;
__device__ __forceinline__ f32x4 treduce8(float (&v)[32], int lane) {
#pragma unroll
  for (int i = 0; i < 16; ++i) v[i] = swap32_add(v[i], v[i + 16]);
#pragma unroll
  for (int i = 0; i < 8; ++i) v[i] = swap16_add(v[i], v[i + 8]);
#pragma unroll
  for (int i = 0; i < 4; ++i) { float t1 = v[i] + dpp<ROW_ROR + 8>(v[i]), t2 = v[i + 4] + dpp<ROW_ROR + 8>(v[i + 4]); v[i] = (lane & 8) ? t2 : t1; }
  return f32x4{v[0], v[1], v[2], v[3]};
}
template <bool FIRST>
__device__ __forceinline__ void rowpair(float (&v)[32], const v4u& w0, const v4u& w1, unsigned x0, unsigned x1, unsigned x2, unsigned x3) {
  const unsigned xp[4] = {x0, x1, x2, x3};
#pragma unroll
  for (int d = 0; d < 4; ++d) {
    const unsigned lo = __builtin_amdgcn_perm(w1[d], w0[d], 0x05040100u), hi = __builtin_amdgcn_perm(w1[d], w0[d], 0x07060302u);
#pragma unroll
    for (int b = 0; b < 4; ++b) {
      v[b * 8 + 2 * d] = dot2(lo, xp[b], FIRST ? 0.f : v[b * 8 + 2 * d]);
      v[b * 8 + 2 * d + 1] = dot2(hi, xp[b], FIRST ? 0.f : v[b * 8 + 2 * d + 1]);
    }
  }
}
__device__ __forceinline__ f32x4 mfma32(v4u a, v4u b, f32x4 c) {
  return __builtin_amdgcn_mfma_f32_16x16x32_bf16(__builtin_bit_cast(bf16x8, a), __builtin_bit_cast(bf16x8, b), c, 0, 0, 0);
}
__device__ __forceinline__ f32x4 mfma16(v2u a, v2u b, f32x4 c) {
  return __builtin_amdgcn_mfma_f32_16x16x16bf16_1k(__builtin_bit_cast(s16x4, a), __builtin_bit_cast(s16x4, b), c, 0, 0, 0);
}
__device__ __forceinline__ float sel4(const f32x4& d, int g) { return g == 0 ? d[0] : g == 1 ? d[1] : g == 2 ? d[2] : d[3]; }
__device__ __forceinline__ unsigned lds_inc(LAS unsigned char* p, int lane) {
  unsigned old = 0;
  if (lane == 0) old = __hip_atomic_fetch_add((LAS unsigned*)p, 1u, __ATOMIC_RELAXED, __HIP_MEMORY_SCOPE_WORKGROUP);
  return __builtin_amdgcn_readfirstlane(old);
}
__device__ __forceinline__ unsigned emb(float v, unsigned t8) { return ((__float_as_uint(v) + 0x80u) & 0xffffff00u) | t8; }
__device__ __forceinline__ bool emb_ok(const v4u& a, unsigned t8) { return (((a.x ^ t8) | (a.y ^ t8) | (a.z ^ t8) | (a.w ^ t8)) & 0xffu) == 0u; }
__device__ __forceinline__ float unemb(unsigned u) { return __uint_as_float(u & 0xffffff00u); }
__device__ __forceinline__ void tr8x4(const v2u (&r)[8], v4u (&f)[4]) {
#pragma unroll
  for (int i = 0; i < 4; ++i) {
    f[0][i] = __builtin_amdgcn_perm(r[2 * i + 1].x, r[2 * i].x, 0x05040100u); f[1][i] = __builtin_amdgcn_perm(r[2 * i + 1].x, r[2 * i].x, 0x07060302u);
    f[2][i] = __builtin_amdgcn_perm(r[2 * i + 1].y, r[2 * i].y, 0x05040100u); f[3][i] = __builtin_amdgcn_perm(r[2 * i + 1].y, r[2 * i].y, 0x07060302u);
  }
}
#define SB() __builtin_amdgcn_sched_barrier(0)
#define CB() asm volatile("" ::: "memory")
#define LGKM0() asm volatile("s_waitcnt lgkmcnt(0)" ::: "memory")
#define BAR() do { LGKM0(); __builtin_amdgcn_s_barrier(); CB(); } while (0)
#define STAMP(i) do { } while (0)

__global__ void __launch_bounds__(512, 2)
fused_kernel(const void* __restrict__ px, const void* __restrict__ pk, const void* __restrict__ pv, const void* __restrict__ pwq,
             const void* __restrict__ pwo, const void* __restrict__ pwg, const void* __restrict__ pwu, const void* __restrict__ pwd,
             const void* __restrict__ pgw, void* __restrict__ pout, void* pws) {
  __shared__ __attribute__((aligned(16))) unsigned char smem[L_END];
  LAS unsigned char* L = (LAS unsigned char*)smem;
  STAMP(0);
  asm volatile("" :: "s"(px), "s"(pk), "s"(pv), "s"(pwq), "s"(pwo), "s"(pwg), "s"(pwu), "s"(pwd), "s"(pgw), "s"(pout), "s"(pws));
  const int tid = threadIdx.x, lane = tid & 63, w = __builtin_amdgcn_readfirstlane(tid >> 6);
  const int g = lane >> 4, c = lane & 15;
  const int blk = blockIdx.x, kvh = blk & 7, r = blk >> 3;
  const int b_att = r >> 3, s_att = r & 7;
  const int rb = r >> 2, cq = r & 3;
  const int pidx = kvh * 8 + rb;

  const size_t kvoff = ((size_t)(b_att * 8 + kvh) * 8192 + s_att * 1024) * 256;
  const rsrc_t rk = mk((const char*)pk + kvoff, 262144), rv = mk((const char*)pv + kvoff, 262144);
  const rsrc_t rws = mk(pws, WS_END);
  const rsrc_t rx = mk(px, 4096), rgw = mk(pgw, 1024);
  const rsrc_t rwo = mk(pwo, 4096 * 512 * 2), rwd = mk(pwd, 1792 * 512 * 2);

  __builtin_amdgcn_s_setprio(3);
  const bool isq = r < 16;
  const int gu = kvh * 14 + (r - 16);
  const int um = gu >= 56 ? 1 : 0;
  const int ulc = isq ? kvh * 8 + (r >> 1) : ((gu - 56 * um) >> 1);
  const int uh = isq ? (r & 1) : (gu & 1);
  v4u wt[4]; v2u xq[4];
  {
    const int row = uh * 256 + w * 32 + 4 * (lane >> 3);
    const unsigned rstr = isq ? 8192u : 3584u;
    const rsrc_t ru = mk(isq ? pwq : (um ? pwu : pwg), isq ? 512 * 4096 * 2 : 512 * 1792 * 2);
    const unsigned uo = r < 30 ? (unsigned)row * rstr + (unsigned)(ulc * 128 + (lane & 7) * 16) : OOB;
#pragma unroll
    for (int i = 0; i < 4; ++i) wt[i] = __builtin_amdgcn_raw_buffer_load_b128(ru, uo + i * rstr, 0, W_AUX);
#pragma unroll
    for (int b = 0; b < 4; ++b) xq[b] = __builtin_amdgcn_raw_buffer_load_b64(rx, row * 2 + b * 1024, 0, X_AUX);
  }
  SB();
  if (tid == 0) { *(volatile LAS v4u*)(L + L_FLAG) = v4u{0u, 0u, 0u, 0u}; *(volatile LAS v4u*)(L + L_FLAG2) = v4u{0u, 0u, 0u, 0u}; }
  BAR();
  SB();
  const unsigned ko = c * 256 + g * 16, vo = g * 1024 + c * 16;
  v4u kb[NBUF][4], vb[NBUF][4];
  auto issueK = [&](int j, int i0, int i1) {
    const unsigned so = (unsigned)(j * 8 + w) * 4096u;
#pragma unroll
    for (int i = i0; i < i1; ++i) kb[j % NBUF][i] = __builtin_amdgcn_raw_buffer_load_b128(rk, ko + i * 64, so, K_AUX);
    SB();
  };
  auto issueV = [&](int j, int i0, int i1) {
    const unsigned so = (unsigned)(j * 8 + w) * 4096u;
#pragma unroll
    for (int i = i0; i < i1; ++i) vb[j % NBUF][i] = __builtin_amdgcn_raw_buffer_load_b128(rv, vo + i * 256, so, V_AUX);
    SB();
  };
  auto issue = [&](int j) { issueK(j, 0, 4); issueV(j, 0, 4); STAMP(11 + j); };
  const unsigned E = *(const __attribute__((address_space(4))) unsigned*)pws;
  SB();
  issueV(0, 0, PREV);
  issueK(0, 0, PREK);
  const unsigned tag = E * 0x9E3779B5u + 0x7F4A7C15u;
  const unsigned t8 = tag & 0xffu;
  {
    LAS f32x4* red = (LAS f32x4*)(L + L_RED);
    float v[32];
    rowpair<true>(v, wt[0], wt[1], xq[0].x, xq[1].x, xq[2].x, xq[3].x);
    rowpair<false>(v, wt[2], wt[3], xq[0].y, xq[1].y, xq[2].y, xq[3].y);
    red[w * 64 + lane] = treduce8(v, lane);
    LGKM0();
    const unsigned old = lds_inc(L + L_FLAG + 4, lane);
    CB();
    const f32x4 s = ((red[lane] + red[64 + lane]) + (red[128 + lane] + red[192 + lane])) + ((red[256 + lane] + red[320 + lane]) + (red[384 + lane] + red[448 + lane]));
    const int c64 = (lane & 7) * 8 + ((lane >> 3) & 1) * 4;
    const unsigned po = isq ? WS_QG + (unsigned)((((kvh * 4 + g) * 2 + uh) * 512 + (r >> 1) * 64 + c64) * 4)
                            : WS_GU + (unsigned)((((um * 2 + uh) * 4 + g) * 1792 + ulc * 64 + c64) * 4);
    __builtin_amdgcn_raw_buffer_store_b128(v4u{emb(s[0], t8), emb(s[1], t8), emb(s[2], t8), emb(s[3], t8)}, rws, (old == 7 && r < 30) ? po : OOB, 0, AUX_SC1);
    SB();
    __builtin_amdgcn_s_setprio(0);
    SB();
  }
  issueV(0, PREV, 4);
  issueK(0, PREK, 4);
  STAMP(11);
#pragma unroll
  for (int j = 1; j < QPRE; ++j) issue(j);
  const unsigned qgo = w < 2 ? WS_QG + (unsigned)((kvh * 4 + b_att) * 4096 + w * 1024 + lane * 16) : OOB;
  CB();
  v4u qa = __builtin_amdgcn_raw_buffer_load_b128(rws, qgo, 0, AUX_SC1);
  v4u qb = __builtin_amdgcn_raw_buffer_load_b128(rws, qgo + 2048, 0, AUX_SC1);
  SB();
  issue(QPRE);
  if (w < 2) {
    if (__builtin_amdgcn_ballot_w64(emb_ok(qa, t8) && emb_ok(qb, t8)) != ~0ull) {
      for (unsigned spin = 0; spin < SPIN_CAP; ++spin) {
        CB();
        qa = __builtin_amdgcn_raw_buffer_load_b128(rws, qgo, 0, AUX_SC1);
        qb = __builtin_amdgcn_raw_buffer_load_b128(rws, qgo + 2048, 0, AUX_SC1);
        if (__builtin_amdgcn_ballot_w64(emb_ok(qa, t8) && emb_ok(qb, t8)) == ~0ull) break;
      }
    }
    *(LAS v2u*)(L + L_Q + (w * 128 + lane * 2) * 4) = v2u{pk_bf16(unemb(qa.x) + unemb(qb.x), unemb(qa.y) + unemb(qb.y)), pk_bf16(unemb(qa.z) + unemb(qb.z), unemb(qa.w) + unemb(qb.w))};
    LGKM0();
    (void)lds_inc(L + L_FLAG, lane);
    STAMP(9);
  }
  SB();
#pragma unroll
  for (int j = QPRE + 1; j < NBUF; ++j) issue(j);
  for (unsigned spin = 0; spin < (SPIN_CAP << 4); ++spin) {
    if (*(volatile LAS unsigned*)(L + L_FLAG) >= 2u) break;
    __builtin_amdgcn_s_sleep(1);
  }
  CB();
  STAMP(2);
  v4u qf[4];
#pragma unroll
  for (int i = 0; i < 4; ++i) qf[i] = *(LAS v4u*)(L + L_Q + (((c & 3) * 64 + 16 * i + 4 * g) * 4));

  constexpr float C2 = 0.08838834764831845f * 1.4426950408889634f;
  f32x4 acc[8];
#pragma unroll
  for (int e = 0; e < 8; ++e) acc[e] = f32x4{0.f, 0.f, 0.f, 0.f};
  float m = -INFINITY, l = 0.f;
#pragma unroll
  for (int j = 0; j < 8; ++j) {
    const int bf = j % NBUF;
    f32x4 sc = {0.f, 0.f, 0.f, 0.f};
#pragma unroll
    for (int i = 0; i < 4; ++i) sc = mfma32(kb[bf][i], qf[i], sc);
    const float t0 = sc[0] * C2, t1 = sc[1] * C2, t2 = sc[2] * C2, t3 = sc[3] * C2;
    const float cm = xrow_max(fmaxf(fmaxf(t0, t1), fmaxf(t2, t3)));
    const float mn = fmaxf(m, cm);
    const float alpha = __builtin_amdgcn_exp2f(m - mn);
    const float p0 = __builtin_amdgcn_exp2f(t0 - mn), p1 = __builtin_amdgcn_exp2f(t1 - mn), p2 = __builtin_amdgcn_exp2f(t2 - mn), p3 = __builtin_amdgcn_exp2f(t3 - mn);
    l = l * alpha + ((p0 + p1) + (p2 + p3));
    m = mn;
    const v2u pa = {pk_bf16(p0, p1), pk_bf16(p2, p3)};
    if (j > 0) {
      const float a0 = dpp<0x00>(alpha), a1 = dpp<0x55>(alpha), a2 = dpp<0xAA>(alpha), a3 = dpp<0xFF>(alpha);
#pragma unroll
      for (int e = 0; e < 8; ++e) { acc[e][0] *= a0; acc[e][1] *= a1; acc[e][2] *= a2; acc[e][3] *= a3; }
    }
#pragma unroll
    for (int d = 0; d < 4; ++d) {
      const v2u lo = {__builtin_amdgcn_perm(vb[bf][1][d], vb[bf][0][d], 0x05040100u), __builtin_amdgcn_perm(vb[bf][3][d], vb[bf][2][d], 0x05040100u)};
      const v2u hi = {__builtin_amdgcn_perm(vb[bf][1][d], vb[bf][0][d], 0x07060302u), __builtin_amdgcn_perm(vb[bf][3][d], vb[bf][2][d], 0x07060302u)};
      acc[2 * d] = mfma16(pa, lo, acc[2 * d]);
      acc[2 * d + 1] = mfma16(pa, hi, acc[2 * d + 1]);
    }
    SB();
    if (j + NBUF < 8) issue(j + NBUF);
    if (j + NBUF == 7) { if (lane == 0) (void)__hip_atomic_fetch_add((LAS unsigned*)(L + L_FLAG + 8), 1u, __ATOMIC_RELAXED, __HIP_MEMORY_SCOPE_WORKGROUP); SB(); }
  }
  STAMP(3);
  l = xrow_sum(l);
  if (g == 0) {
    LAS float* mo = (LAS float*)(L + L_MRG) + w * 512 + 8 * c;
#pragma unroll
    for (int h = 0; h < 4; ++h) {
      *(LAS f32x4*)(mo + h * 128) = f32x4{acc[0][h], acc[1][h], acc[2][h], acc[3][h]};
      *(LAS f32x4*)(mo + h * 128 + 4) = f32x4{acc[4][h], acc[5][h], acc[6][h], acc[7][h]};
    }
    if (c < 4) *(LAS f32x2*)(L + L_ML + (c * 8 + w) * 8) = f32x2{m, l};
  }
  LGKM0();
  (void)lds_inc(L + L_FLAG + 12, lane);
  auto lds_wait = [&](int off, unsigned n) {
    for (unsigned spin = 0; spin < (SPIN_CAP << 4); ++spin) {
      if (*(volatile LAS unsigned*)(L + off) >= n) break;
      __builtin_amdgcn_s_sleep(WSLP);
    }
    CB();
  };
  const f32x4 zero = {0.f, 0.f, 0.f, 0.f};
  if (w < 2) {
    for (unsigned spin = 0; spin < (SPIN_CAP << 4); ++spin) {
      if (*(volatile LAS unsigned*)(L + L_FLAG + 8) >= DTH) break;
      __builtin_amdgcn_s_sleep(1);
    }
    CB();
    v2u r0[8], r1[8], r2[8];
    v4u bo[2][4], bd[4];
    const unsigned colb = (unsigned)(cq * 128 + w * 64 + 4 * c) * 2u;
    const unsigned wdb = (unsigned)(pidx * 28 + 8 * g) * 1024u + colb;
#pragma unroll
    for (int i = 0; i < 8; ++i) r2[i] = __builtin_amdgcn_raw_buffer_load_b64(rwd, (8 * g + i < 28) ? wdb + i * 1024 : OOB, 0, W_AUX);
    const unsigned wob = (unsigned)(kvh * 512 + rb * 64 + 8 * g) * 1024u + colb;
#pragma unroll
    for (int i = 0; i < 8; ++i) r0[i] = __builtin_amdgcn_raw_buffer_load_b64(rwo, wob + i * 1024, 0, W_AUX);
#pragma unroll
    for (int i = 0; i < 8; ++i) r1[i] = __builtin_amdgcn_raw_buffer_load_b64(rwo, wob + 32768 + i * 1024, 0, W_AUX);
    SB();
    unsigned fgo = WS_FG + (unsigned)((((g * 16 + cq * 4 + w * 2 + (c >> 3)) * 64 + pidx) * 32 + (c & 7) * 4) * 4);
    lds_wait(L_FLAG2, 1u);
    const v4u hf = *(LAS v4u*)(L + L_H + ((c >> 2) * 32 + 8 * g) * 2);
    tr8x4(r2, bd);
    f32x4 db[4];
#pragma unroll
    for (int e = 0; e < 4; ++e) db[e] = mfma32(hf, bd[e], zero);
    tr8x4(r0, bo[0]); tr8x4(r1, bo[1]);
#pragma unroll
    for (int e = 0; e < 4; ++e) asm volatile("" : "+v"(bo[0][e]), "+v"(bo[1][e]), "+v"(db[e]));
    asm volatile("" : "+v"(fgo));
    STAMP(10);
    lds_wait(L_FLAG2 + 4, 4u);
    STAMP(5);
    const v4u of0 = *(LAS v4u*)(L + L_O + ((c >> 2) * 64 + 8 * g) * 2), of1 = *(LAS v4u*)(L + L_O + ((c >> 2) * 64 + 32 + 8 * g) * 2);
#pragma unroll
    for (int e = 0; e < 4; ++e) db[e] = mfma32(of0, bo[0][e], db[e]);
#pragma unroll
    for (int e = 0; e < 4; ++e) db[e] = mfma32(of1, bo[1][e], db[e]);
    __builtin_amdgcn_raw_buffer_store_b128(v4u{emb(db[0][0], t8), emb(db[1][0], t8), emb(db[2][0], t8), emb(db[3][0], t8)}, rws, fgo, 0, AUX_SC1);
    STAMP(6);
  } else if (w == 2) {
    const unsigned go = c < 7 ? WS_GU + (unsigned)((g * 1792 + pidx * 28 + c * 4) * 4) : OOB;
    v4u xv[4];
#pragma unroll
    for (int b = 0; b < 4; ++b) xv[b] = __builtin_amdgcn_raw_buffer_load_b128(rx, lane * 16 + b * 1024, 0, 0);
    const v4u gv = __builtin_amdgcn_raw_buffer_load_b128(rgw, lane * 16, 0, 0);
    v4u gq[4];
    for (unsigned spin = 0; spin < SPIN_CAP; ++spin) {
      CB();
#pragma unroll
      for (int i = 0; i < 4; ++i) gq[i] = __builtin_amdgcn_raw_buffer_load_b128(rws, go + i * 28672, 0, AUX_SC1);
      SB();
      if (__builtin_amdgcn_ballot_w64(c >= 7 || (emb_ok(gq[0], t8) && emb_ok(gq[1], t8) && emb_ok(gq[2], t8) && emb_ok(gq[3], t8))) == ~0ull) break;
      __builtin_amdgcn_s_sleep(2);
    }
    float a[4];
#pragma unroll
    for (int b = 0; b < 4; ++b) { a[b] = dot2(xv[b].x, gv.x, 0.f); a[b] = dot2(xv[b].y, gv.y, a[b]); a[b] = dot2(xv[b].z, gv.z, a[b]); a[b] = dot2(xv[b].w, gv.w, a[b]); }
    a[0] = swap32_add(a[0], a[2]); a[1] = swap32_add(a[1], a[3]);
    a[0] = swap16_add(a[0], a[1]);
    a[0] += dpp<ROW_ROR + 8>(a[0]); a[0] += dpp<ROW_ROR + 4>(a[0]); a[0] += dpp<ROW_ROR + 2>(a[0]); a[0] += dpp<ROW_ROR + 1>(a[0]);
    const float gt = 1.f / (1.f + __expf(-a[0]));
    if (c == 0) *(LAS float*)(L + L_GATE + g * 4) = gt;
    float hh_[4];
#pragma unroll
    for (int i = 0; i < 4; ++i) hh_[i] = (1.f - gt) * (unemb(gq[2][i]) + unemb(gq[3][i])) / (1.f + __expf(-(unemb(gq[0][i]) + unemb(gq[1][i]))));
    if (c < 8) *(LAS v2u*)(L + L_H + (g * 16 + c * 2) * 4) = c < 7 ? v2u{pk_bf16(hh_[0], hh_[1]), pk_bf16(hh_[2], hh_[3])} : v2u{0u, 0u};
    LGKM0();
    if (lane == 0) *(volatile LAS unsigned*)(L + L_FLAG2) = 1u;
  } else if (w >= 4) {
    const int v = w - 4;
    const int sp = lane >> 3;
    unsigned pgo = WS_PG + (unsigned)(((kvh * 32 + r) * 512 + v * 128 + lane * 2) * 4);
    unsigned po = WS_PG + (unsigned)(((kvh * 32 + v * 8 + sp) * 512 + rb * 64 + (lane & 7) * 4) * 4);
    asm volatile("" : "+v"(pgo), "+v"(po));
    lds_wait(L_FLAG + 12, 8u);
    {
      f32x4 ml[4]; f32x2 ov[8];
#pragma unroll
      for (int i = 0; i < 4; ++i) ml[i] = *(LAS f32x4*)(L + L_ML + v * 64 + i * 16);
#pragma unroll
      for (int i = 0; i < 8; ++i) ov[i] = *(LAS f32x2*)(L + L_MRG + (i * 512 + v * 128 + lane * 2) * 4);
      SB();
      const float M = fmaxf(fmaxf(fmaxf(ml[0][0], ml[0][2]), fmaxf(ml[1][0], ml[1][2])), fmaxf(fmaxf(ml[2][0], ml[2][2]), fmaxf(ml[3][0], ml[3][2])));
      float Ls = 0.f; f32x2 o = {0.f, 0.f};
#pragma unroll
      for (int i = 0; i < 4; ++i) {
        const float w0 = __builtin_amdgcn_exp2f(ml[i][0] - M), w1 = __builtin_amdgcn_exp2f(ml[i][2] - M);
        Ls += w0 * ml[i][1] + w1 * ml[i][3]; o += w0 * ov[2 * i] + w1 * ov[2 * i + 1];
      }
      o *= __builtin_amdgcn_rcpf(Ls);
      const unsigned lseu = __float_as_uint(M + __builtin_amdgcn_logf(Ls));
      const unsigned nb2 = lseu >> ((lane & 3) * 8);
      const unsigned t4 = t8 & 0x0fu;
      __builtin_amdgcn_raw_buffer_store_b64(v2u{emb(o.x, t4 | ((nb2 & 15u) << 4)), emb(o.y, t4 | (nb2 & 0xf0u))}, rws, (EXP_HALF && (lane & 16)) ? OOB : pgo, 0, AUX_SC1);
      SB();
    }
    STAMP(4);
    {
      v4u pl0 = {0, 0, 0, 0}, pl1 = {0, 0, 0, 0};
      bool pok = false;
      auto chk1 = [&](const v4u& x, const v4u& y, bool live) -> bool {
        const bool mine = live && ((((x.x ^ t8) | (x.y ^ t8) | (x.z ^ t8) | (x.w ^ t8)) | ((y.x ^ t8) | (y.y ^ t8) | (y.z ^ t8) | (y.w ^ t8))) & 0x0fu) == 0u;
        const unsigned long long bm = __builtin_amdgcn_ballot_w64(mine);
        const bool take = ((unsigned)(bm >> (lane & 56)) & 0xffu) == 0xffu;
#pragma unroll
        for (int i = 0; i < 4; ++i) { pl0[i] = take ? x[i] : pl0[i]; pl1[i] = take ? y[i] : pl1[i]; }
        pok = pok || take;
        return __builtin_amdgcn_ballot_w64(!pok) == 0ull;
      };
      lds_wait(L_FLAG2, 1u);
      const float gate = *(LAS float*)(L + L_GATE + v * 4);
      for (unsigned spin = 0; spin < SPIN_CAP; ++spin) {
        CB();
        const bool lv = !pok;
        const unsigned o_ = lv ? po : OOB;
        const v4u a0 = __builtin_amdgcn_raw_buffer_load_b128(rws, o_, 0, AUX_SC1);
        const v4u a1 = EXP_HALF ? a0 : __builtin_amdgcn_raw_buffer_load_b128(rws, o_ + 128, 0, AUX_SC1);
        SB();
        if (chk1(a0, a1, lv)) break;
      }
      const unsigned half16 = ((pl0.x >> 4) & 15u) | (pl0.y & 0xf0u) | ((pl0.z & 0xf0u) << 4) | ((pl0.w & 0xf0u) << 8);
      const unsigned other = __builtin_amdgcn_mov_dpp(half16, 0xB1, 0xf, 0xf, true);
      const float lse = __uint_as_float((lane & 1) ? (other | (half16 << 16)) : (half16 | (other << 16)));
      float M = fmaxf(lse, dpp<ROW_ROR + 8>(lse)); M = xrow_max(M);
      const float wt_ = __builtin_amdgcn_exp2f(lse - M);
      float W = wt_; W += dpp<ROW_ROR + 8>(W); W = xrow_sum(W);
      float u[4];
#pragma unroll
      for (int i = 0; i < 4; ++i) u[i] = swap32_add(wt_ * unemb(pl0[i]), wt_ * unemb(pl1[i]));
      u[0] = swap16_add(u[0], u[2]); u[1] = swap16_add(u[1], u[3]);
      u[0] += dpp<ROW_ROR + 8>(u[0]); u[1] += dpp<ROW_ROR + 8>(u[1]);
      const float inv = gate * __builtin_amdgcn_rcpf(W);
      if ((lane & 8) == 0) *(LAS unsigned*)(L + L_O + (v * 64 + (lane >> 5) * 32 + (lane & 7) * 4 + ((lane >> 4) & 1) * 2) * 2) = pk_bf16(u[0] * inv, u[1] * inv);
      LGKM0();
      (void)lds_inc(L + L_FLAG2 + 4, lane);
      STAMP(1);
    }
    if ((r & 3) == 0) {
      const int R = (r >> 2) * 8 + kvh;
      const unsigned fo = WS_FG + (unsigned)(R * 8192 + v * 2048 + lane * 16);
      v4u pl0 = {0, 0, 0, 0}, pl1 = {0, 0, 0, 0};
      bool ok0 = false, ok1 = false;
      auto chk2 = [&](const v4u& x, const v4u& y, bool l0, bool l1) -> bool {
        const bool t0_ = l0 && emb_ok(x, t8), t1_ = l1 && emb_ok(y, t8);
#pragma unroll
        for (int i = 0; i < 4; ++i) { pl0[i] = t0_ ? x[i] : pl0[i]; pl1[i] = t1_ ? y[i] : pl1[i]; }
        ok0 = ok0 || t0_; ok1 = ok1 || t1_;
        return __builtin_amdgcn_ballot_w64(!(ok0 && ok1)) == 0ull;
      };
      for (unsigned spin = 0; spin < SPIN_CAP; ++spin) {
        CB();
        const bool l0 = !ok0, l1 = !ok1;
        const v4u c0 = __builtin_amdgcn_raw_buffer_load_b128(rws, l0 ? fo : OOB, 0, AUX_SC1);
        const v4u c1 = __builtin_amdgcn_raw_buffer_load_b128(rws, l1 ? fo + 1024 : OOB, 0, AUX_SC1);
        SB();
        if (chk2(c0, c1, l0, l1)) break;
      }
      float v0 = unemb(pl0.x) + unemb(pl1.x), v1 = unemb(pl0.y) + unemb(pl1.y), v2 = unemb(pl0.z) + unemb(pl1.z), v3 = unemb(pl0.w) + unemb(pl1.w);
      v0 = swap32_add(v0, v2); v1 = swap32_add(v1, v3);
      v0 = swap16_add(v0, v0); v1 = swap16_add(v1, v1);
      v0 += dpp<ROW_ROR + 8>(v0); v1 += dpp<ROW_ROR + 8>(v1);
      if ((lane & 24) == 0) *(LAS f32x2*)(L + L_RED + (v * 32 + (lane & 7) * 4 + (lane >> 5) * 2) * 4) = f32x2{v0, v1};
      LGKM0();
      const unsigned old = lds_inc(L + L_FLAG2 + 8, lane);
      CB();
      if (old == 3 && lane < 16) {
        const LAS f32x2* fr = (const LAS f32x2*)(L + L_RED) + lane;
        const f32x2 s = (fr[0] + fr[16]) + (fr[32] + fr[48]);
        __builtin_amdgcn_raw_buffer_store_b32(pk_bf16(s.x, s.y), mk(pout, 4096), (R * 16 + lane) * 4, 0, 0);
        __builtin_amdgcn_raw_buffer_store_b32(tag, rws, (blk == 0 && lane == 0) ? 0u : OOB, 0, AUX_SC1);
      }
    }
  }
  STAMP(7);
}
}

extern "C" void kernel_launch(void* const* d_in, const int* in_sizes, int n_in, void* d_out, int out_size, void* d_ws,
                              size_t ws_size, hipStream_t stream) {
  fk::fused_kernel<<<256, 512, 0, stream>>>(d_in[0], d_in[1], d_in[2], d_in[3], d_in[4], d_in[6], d_in[7], d_in[8], d_in[9], d_out, d_ws);
}
```

```cpp
#define SLP1 0
#define SLP2 0
#define WSLP 1
#define W_AUX 0
#define KJM 0
#define VJM 0
#include <hip/hip_runtime.h>
#include <cstdint>

#define K_AUX 2
#define V_AUX 2
#define DEFER 1
#define PIPE 1
#define GAP1 0
#define GAP2 0
#define DTH 8u
#define EXP_HALF 0
#define NBUF 5
#define STAMPS 0
#define QPRE 4
#define PREK 4
#define PREV 4
#define EXP_NOTILE 0
#define EXP_NOTW 0
#define EXP_NOX 0
#define EXP_NOQ 0
#define X_AUX 0
#define SPEC 2

typedef unsigned v4u __attribute__((ext_vector_type(4)));
typedef unsigned v2u __attribute__((ext_vector_type(2)));
typedef float f32x4 __attribute__((ext_vector_type(4)));
typedef float f32x2 __attribute__((ext_vector_type(2)));
typedef short s16x4 __attribute__((ext_vector_type(4)));
typedef __bf16 bf16x8 __attribute__((ext_vector_type(8)));
typedef __bf16 bf16x2 __attribute__((ext_vector_type(2)));
typedef __amdgpu_buffer_rsrc_t rsrc_t;
#define LAS __attribute__((address_space(3)))

namespace fk {
constexpr int AUX_NT = 2, AUX_SC1 = 16;
constexpr unsigned OOB = 0x40000000u;
constexpr unsigned WS_QG = 4096;
constexpr unsigned WS_GU = WS_QG + 131072;
constexpr unsigned WS_PG = WS_GU + 114688;
constexpr unsigned WS_FG = WS_PG + 524288;
constexpr unsigned WS_END = WS_FG + 524288;
constexpr unsigned WS_STAMP = 8u << 20;
constexpr unsigned SPIN_CAP = 1u << 17;

constexpr int L_RED = 0;
constexpr int L_Q = 8192;
constexpr int L_H = L_Q + 1024;
constexpr int L_GATE = L_H + 256;
constexpr int L_FLAG = L_GATE + 16;
constexpr int L_FLAG2 = L_FLAG + 16;
constexpr int L_O = L_FLAG2 + 16;
constexpr int L_ML = L_O + 512;
constexpr int L_MRG = L_ML + 256;
constexpr int L_END = L_MRG + 16384;

__device__ __forceinline__ rsrc_t mk(const void* p, unsigned bytes) {
  return __builtin_amdgcn_make_buffer_rsrc((void*)p, 0, (int)bytes, 0x00020000);
}
__device__ __forceinline__ float dot2(unsigned a, unsigned b, float c) {
  return __builtin_amdgcn_fdot2_f32_bf16(__builtin_bit_cast(bf16x2, a), __builtin_bit_cast(bf16x2, b), c, false);
}
__device__ __forceinline__ unsigned pk_bf16(float lo, float hi) {
  f32x2 v = {lo, hi};
  return __builtin_bit_cast(unsigned, __builtin_convertvector(v, bf16x2));
}
template <int CTRL> __device__ __forceinline__ float dpp(float x) {
  return __builtin_bit_cast(float, __builtin_amdgcn_mov_dpp(__builtin_bit_cast(int, x), CTRL, 0xf, 0xf, true));
}
__device__ __forceinline__ float swap32_add(float a, float b) {
  auto s = __builtin_amdgcn_permlane32_swap(__float_as_uint(a), __float_as_uint(b), false, false);
  return __uint_as_float(s[0]) + __uint_as_float(s[1]);
}
__device__ __forceinline__ float swap16_add(float a, float b) {
  auto s = __builtin_amdgcn_permlane16_swap(__float_as_uint(a), __float_as_uint(b), false, false);
  return __uint_as_float(s[0]) + __uint_as_float(s[1]);
}
__device__ __forceinline__ float xrow_max(float x) {
  auto s = __builtin_amdgcn_permlane16_swap(__float_as_uint(x), __float_as_uint(x), false, false);
  x = fmaxf(__uint_as_float(s[0]), __uint_as_float(s[1]));
  auto t = __builtin_amdgcn_permlane32_swap(__float_as_uint(x), __float_as_uint(x), false, false);
  return fmaxf(__uint_as_float(t[0]), __uint_as_float(t[1]));
}
__device__ __forceinline__ float xrow_sum(float x) {
  x = swap16_add(x, x);
  return swap32_add(x, x);
}
constexpr int ROW_SHL = 0x100, ROW_SHR = 0x110, ROW_ROR = 0x120;
__device__ __forceinline__ f32x4 treduce8(float (&v)[32], int lane) {
#pragma unroll
  for (int i = 0; i < 16; ++i) v[i] = swap32_add(v[i], v[i + 16]);
#pragma unroll
  for (int i = 0; i < 8; ++i) v[i] = swap16_add(v[i], v[i + 8]);
#pragma unroll
  for (int i = 0; i < 4; ++i) { float t1 = v[i] + dpp<ROW_ROR + 8>(v[i]), t2 = v[i + 4] + dpp<ROW_ROR + 8>(v[i + 4]); v[i] = (lane & 8) ? t2 : t1; }
  return f32x4{v[0], v[1], v[2], v[3]};
}
template <bool FIRST>
__device__ __forceinline__ void rowpair(float (&v)[32], const v4u& w0, const v4u& w1, unsigned x0, unsigned x1, unsigned x2, unsigned x3) {
  const unsigned xp[4] = {x0, x1, x2, x3};
#pragma unroll
  for (int d = 0; d < 4; ++d) {
    const unsigned lo = __builtin_amdgcn_perm(w1[d], w0[d], 0x05040100u), hi = __builtin_amdgcn_perm(w1[d], w0[d], 0x07060302u);
#pragma unroll
    for (int b = 0; b < 4; ++b) {
      v[b * 8 + 2 * d] = dot2(lo, xp[b], FIRST ? 0.f : v[b * 8 + 2 * d]);
      v[b * 8 + 2 * d + 1] = dot2(hi, xp[b], FIRST ? 0.f : v[b * 8 + 2 * d + 1]);
    }
  }
}
__device__ __forceinline__ f32x4 mfma32(v4u a, v4u b, f32x4 c) {
  return __builtin_amdgcn_mfma_f32_16x16x32_bf16(__builtin_bit_cast(bf16x8, a), __builtin_bit_cast(bf16x8, b), c, 0, 0, 0);
}
__device__ __forceinline__ f32x4 mfma16(v2u a, v2u b, f32x4 c) {
  return __builtin_amdgcn_mfma_f32_16x16x16bf16_1k(__builtin_bit_cast(s16x4, a), __builtin_bit_cast(s16x4, b), c, 0, 0, 0);
}
__device__ __forceinline__ float sel4(const f32x4& d, int g) { return g == 0 ? d[0] : g == 1 ? d[1] : g == 2 ? d[2] : d[3]; }
__device__ __forceinline__ unsigned lds_inc(LAS unsigned char* p, int lane) {
  unsigned old = 0;
  if (lane == 0) old = __hip_atomic_fetch_add((LAS unsigned*)p, 1u, __ATOMIC_RELAXED, __HIP_MEMORY_SCOPE_WORKGROUP);
  return __builtin_amdgcn_readfirstlane(old);
}
__device__ __forceinline__ unsigned emb(float v, unsigned t8) { return ((__float_as_uint(v) + 0x80u) & 0xffffff00u) | t8; }
__device__ __forceinline__ bool emb_ok(const v4u& a, unsigned t8) { return (((a.x ^ t8) | (a.y ^ t8) | (a.z ^ t8) | (a.w ^ t8)) & 0xffu) == 0u; }
__device__ __forceinline__ unsigned emb_bad(const v4u& a, unsigned t8) { return ((a.x ^ t8) | (a.y ^ t8) | (a.z ^ t8) | (a.w ^ t8)) & 0xffu; }
__device__ __forceinline__ float unemb(unsigned u) { return __uint_as_float(u & 0xffffff00u); }
__device__ __forceinline__ void tr8x4(const v2u (&r)[8], v4u (&f)[4]) {
#pragma unroll
  for (int i = 0; i < 4; ++i) {
    f[0][i] = __builtin_amdgcn_perm(r[2 * i + 1].x, r[2 * i].x, 0x05040100u); f[1][i] = __builtin_amdgcn_perm(r[2 * i + 1].x, r[2 * i].x, 0x07060302u);
    f[2][i] = __builtin_amdgcn_perm(r[2 * i + 1].y, r[2 * i].y, 0x05040100u); f[3][i] = __builtin_amdgcn_perm(r[2 * i + 1].y, r[2 * i].y, 0x07060302u);
  }
}
#define SB() __builtin_amdgcn_sched_barrier(0)
#define CB() asm volatile("" ::: "memory")
#define LGKM0() asm volatile("s_waitcnt lgkmcnt(0)" ::: "memory")
#define BAR() do { LGKM0(); __builtin_amdgcn_s_barrier(); CB(); } while (0)
#define STAMP(i) do { } while (0)

__global__ void __launch_bounds__(512, 2)
fused_kernel(const void* __restrict__ px, const void* __restrict__ pk, const void* __restrict__ pv, const void* __restrict__ pwq,
             const void* __restrict__ pwo, const void* __restrict__ pwg, const void* __restrict__ pwu, const void* __restrict__ pwd,
             const void* __restrict__ pgw, void* __restrict__ pout, void* pws) {
  __shared__ __attribute__((aligned(16))) unsigned char smem[L_END];
  LAS unsigned char* L = (LAS unsigned char*)smem;
  STAMP(0);
  asm volatile("" :: "s"(px), "s"(pk), "s"(pv), "s"(pwq), "s"(pwo), "s"(pwg), "s"(pwu), "s"(pwd), "s"(pgw), "s"(pout), "s"(pws));
  const int tid = threadIdx.x, lane = tid & 63, w = __builtin_amdgcn_readfirstlane(tid >> 6);
  const int g = lane >> 4, c = lane & 15;
  const int blk = blockIdx.x, kvh = blk & 7, r = blk >> 3;
  const int b_att = r >> 3, s_att = r & 7;
  const int rb = r >> 2, cq = r & 3;
  const int pidx = kvh * 8 + rb;

  const size_t kvoff = ((size_t)(b_att * 8 + kvh) * 8192 + s_att * 1024) * 256;
  const rsrc_t rk = mk((const char*)pk + kvoff, 262144), rv = mk((const char*)pv + kvoff, 262144);
  const rsrc_t rws = mk(pws, WS_END);
  const rsrc_t rx = mk(px, 4096), rgw = mk(pgw, 1024);
  const rsrc_t rwo = mk(pwo, 4096 * 512 * 2), rwd = mk(pwd, 1792 * 512 * 2);

  __builtin_amdgcn_s_setprio(3);
  const bool isq = r < 16;
  const int gu = kvh * 14 + (r - 16);
  const int um = gu >= 56 ? 1 : 0;
  const int ulc = isq ? kvh * 8 + (r >> 1) : ((gu - 56 * um) >> 1);
  const int uh = isq ? (r & 1) : (gu & 1);
  v4u wt[4]; v2u xq[4];
  {
    const int row = uh * 256 + w * 32 + 4 * (lane >> 3);
    const unsigned rstr = isq ? 8192u : 3584u;
    const rsrc_t ru = mk(isq ? pwq : (um ? pwu : pwg), isq ? 512 * 4096 * 2 : 512 * 1792 * 2);
    const unsigned uo = r < 30 ? (unsigned)row * rstr + (unsigned)(ulc * 128 + (lane & 7) * 16) : OOB;
#pragma unroll
    for (int i = 0; i < 4; ++i) wt[i] = __builtin_amdgcn_raw_buffer_load_b128(ru, uo + i * rstr, 0, W_AUX);
#pragma unroll
    for (int b = 0; b < 4; ++b) xq[b] = __builtin_amdgcn_raw_buffer_load_b64(rx, row * 2 + b * 1024, 0, X_AUX);
  }
  SB();
  if (tid == 0) { *(volatile LAS v4u*)(L + L_FLAG) = v4u{0u, 0u, 0u, 0u}; *(volatile LAS v4u*)(L + L_FLAG2) = v4u{0u, 0u, 0u, 0u}; }
  BAR();
  SB();
  const unsigned ko = c * 256 + g * 16, vo = g * 1024 + c * 16;
  v4u kb[NBUF][4], vb[NBUF][4];
  auto issueK = [&](int j, int i0, int i1) {
    const unsigned so = (unsigned)(j * 8 + w) * 4096u;
#pragma unroll
    for (int i = i0; i < i1; ++i) kb[j % NBUF][i] = ((KJM >> j) & 1) ? __builtin_amdgcn_raw_buffer_load_b128(rk, ko + i * 64, so, 0) : __builtin_amdgcn_raw_buffer_load_b128(rk, ko + i * 64, so, 2);
    SB();
  };
  auto issueV = [&](int j, int i0, int i1) {
    const unsigned so = (unsigned)(j * 8 + w) * 4096u;
#pragma unroll
    for (int i = i0; i < i1; ++i) vb[j % NBUF][i] = ((VJM >> j) & 1) ? __builtin_amdgcn_raw_buffer_load_b128(rv, vo + i * 256, so, 0) : __builtin_amdgcn_raw_buffer_load_b128(rv, vo + i * 256, so, 2);
    SB();
  };
  auto issue = [&](int j) { issueK(j, 0, 4); issueV(j, 0, 4); STAMP(11 + j); };
  const unsigned E = *(const __attribute__((address_space(4))) unsigned*)pws;
  SB();
  issueV(0, 0, PREV);
  issueK(0, 0, PREK);
  const unsigned tag = E * 0x9E3779B5u + 0x7F4A7C15u;
  const unsigned t8 = tag & 0xffu;
  {
    LAS f32x4* red = (LAS f32x4*)(L + L_RED);
    float v[32];
    rowpair<true>(v, wt[0], wt[1], xq[0].x, xq[1].x, xq[2].x, xq[3].x);
    rowpair<false>(v, wt[2], wt[3], xq[0].y, xq[1].y, xq[2].y, xq[3].y);
    red[w * 64 + lane] = treduce8(v, lane);
    LGKM0();
    const unsigned old = lds_inc(L + L_FLAG + 4, lane);
    CB();
    const f32x4 s = ((red[lane] + red[64 + lane]) + (red[128 + lane] + red[192 + lane])) + ((red[256 + lane] + red[320 + lane]) + (red[384 + lane] + red[448 + lane]));
    const int c64 = (lane & 7) * 8 + ((lane >> 3) & 1) * 4;
    const unsigned po = isq ? WS_QG + (unsigned)((((kvh * 4 + g) * 2 + uh) * 512 + (r >> 1) * 64 + c64) * 4)
                            : WS_GU + (unsigned)((((um * 2 + uh) * 4 + g) * 1792 + ulc * 64 + c64) * 4);
    __builtin_amdgcn_raw_buffer_store_b128(v4u{emb(s[0], t8), emb(s[1], t8), emb(s[2], t8), emb(s[3], t8)}, rws, (old == 7 && r < 30) ? po : OOB, 0, AUX_SC1);
    SB();
    __builtin_amdgcn_s_setprio(0);
    SB();
  }
  issueV(0, PREV, 4);
  issueK(0, PREK, 4);
  STAMP(11);
#pragma unroll
  for (int j = 1; j < QPRE; ++j) issue(j);
  const unsigned qgo = w < 2 ? WS_QG + (unsigned)((kvh * 4 + b_att) * 4096 + w * 1024 + lane * 16) : OOB;
  CB();
  v4u qa = __builtin_amdgcn_raw_buffer_load_b128(rws, qgo, 0, AUX_SC1);
  v4u qb = __builtin_amdgcn_raw_buffer_load_b128(rws, qgo + 2048, 0, AUX_SC1);
  SB();
  issue(QPRE);
  if (w < 2) {
    if (__builtin_amdgcn_ballot_w64((emb_bad(qa, t8) | emb_bad(qb, t8)) != 0u) != 0ull) {
      for (unsigned spin = 0; spin < SPIN_CAP; ++spin) {
        CB();
        qa = __builtin_amdgcn_raw_buffer_load_b128(rws, qgo, 0, AUX_SC1);
        qb = __builtin_amdgcn_raw_buffer_load_b128(rws, qgo + 2048, 0, AUX_SC1);
        if (__builtin_amdgcn_ballot_w64((emb_bad(qa, t8) | emb_bad(qb, t8)) != 0u) == 0ull) break;
      }
    }
    *(LAS v2u*)(L + L_Q + (w * 128 + lane * 2) * 4) = v2u{pk_bf16(unemb(qa.x) + unemb(qb.x), unemb(qa.y) + unemb(qb.y)), pk_bf16(unemb(qa.z) + unemb(qb.z), unemb(qa.w) + unemb(qb.w))};
    LGKM0();
    (void)lds_inc(L + L_FLAG, lane);
    STAMP(9);
  }
  SB();
#pragma unroll
  for (int j = QPRE + 1; j < NBUF; ++j) issue(j);
  for (unsigned spin = 0; spin < (SPIN_CAP << 4); ++spin) {
    if (*(volatile LAS unsigned*)(L + L_FLAG) >= 2u) break;
    __builtin_amdgcn_s_sleep(1);
  }
  CB();
  STAMP(2);
  v4u qf[4];
#pragma unroll
  for (int i = 0; i < 4; ++i) qf[i] = *(LAS v4u*)(L + L_Q + (((c & 3) * 64 + 16 * i + 4 * g) * 4));

  constexpr float C2 = 0.08838834764831845f * 1.4426950408889634f;
  f32x4 acc[8];
#pragma unroll
  for (int e = 0; e < 8; ++e) acc[e] = f32x4{0.f, 0.f, 0.f, 0.f};
  float m = -INFINITY, l = 0.f;
#pragma unroll
  for (int j = 0; j < 8; ++j) {
    const int bf = j % NBUF;
    f32x4 sc = {0.f, 0.f, 0.f, 0.f};
#pragma unroll
    for (int i = 0; i < 4; ++i) sc = mfma32(kb[bf][i], qf[i], sc);
    const float t0 = sc[0] * C2, t1 = sc[1] * C2, t2 = sc[2] * C2, t3 = sc[3] * C2;
    const float cm = xrow_max(fmaxf(fmaxf(t0, t1), fmaxf(t2, t3)));
    const float mn = fmaxf(m, cm);
    const float alpha = __builtin_amdgcn_exp2f(m - mn);
    const float p0 = __builtin_amdgcn_exp2f(t0 - mn), p1 = __builtin_amdgcn_exp2f(t1 - mn), p2 = __builtin_amdgcn_exp2f(t2 - mn), p3 = __builtin_amdgcn_exp2f(t3 - mn);
    l = l * alpha + ((p0 + p1) + (p2 + p3));
    m = mn;
    const v2u pa = {pk_bf16(p0, p1), pk_bf16(p2, p3)};
    if (j > 0) {
      const float a0 = dpp<0x00>(alpha), a1 = dpp<0x55>(alpha), a2 = dpp<0xAA>(alpha), a3 = dpp<0xFF>(alpha);
#pragma unroll
      for (int e = 0; e < 8; ++e) { acc[e][0] *= a0; acc[e][1] *= a1; acc[e][2] *= a2; acc[e][3] *= a3; }
    }
#pragma unroll
    for (int d = 0; d < 4; ++d) {
      const v2u lo = {__builtin_amdgcn_perm(vb[bf][1][d], vb[bf][0][d], 0x05040100u), __builtin_amdgcn_perm(vb[bf][3][d], vb[bf][2][d], 0x05040100u)};
      const v2u hi = {__builtin_amdgcn_perm(vb[bf][1][d], vb[bf][0][d], 0x07060302u), __builtin_amdgcn_perm(vb[bf][3][d], vb[bf][2][d], 0x07060302u)};
      acc[2 * d] = mfma16(pa, lo, acc[2 * d]);
      acc[2 * d + 1] = mfma16(pa, hi, acc[2 * d + 1]);
    }
    if (j == QPRE) asm volatile("" :: "v"(qa), "v"(qb));
    SB();
    if (j + NBUF < 8) issue(j + NBUF);
    if (j + NBUF == 7) { if (lane == 0) (void)__hip_atomic_fetch_add((LAS unsigned*)(L + L_FLAG + 8), 1u, __ATOMIC_RELAXED, __HIP_MEMORY_SCOPE_WORKGROUP); SB(); }
  }
  STAMP(3);
  l = xrow_sum(l);
  if (g == 0) {
    LAS float* mo = (LAS float*)(L + L_MRG) + w * 512 + 8 * c;
#pragma unroll
    for (int h = 0; h < 4; ++h) {
      *(LAS f32x4*)(mo + h * 128) = f32x4{acc[0][h], acc[1][h], acc[2][h], acc[3][h]};
      *(LAS f32x4*)(mo + h * 128 + 4) = f32x4{acc[4][h], acc[5][h], acc[6][h], acc[7][h]};
    }
    if (c < 4) *(LAS f32x2*)(L + L_ML + (c * 8 + w) * 8) = f32x2{m, l};
  }
  LGKM0();
  (void)lds_inc(L + L_FLAG + 12, lane);
  auto lds_wait = [&](int off, unsigned n) {
    for (unsigned spin = 0; spin < (SPIN_CAP << 4); ++spin) {
      if (*(volatile LAS unsigned*)(L + off) >= n) break;
      __builtin_amdgcn_s_sleep(WSLP);
    }
    CB();
  };
  const f32x4 zero = {0.f, 0.f, 0.f, 0.f};
  if (w < 2) {
    for (unsigned spin = 0; spin < (SPIN_CAP << 4); ++spin) {
      if (*(volatile LAS unsigned*)(L + L_FLAG + 8) >= DTH) break;
      __builtin_amdgcn_s_sleep(1);
    }
    CB();
    v2u r0[8], r1[8], r2[8];
    v4u bo[2][4], bd[4];
    const unsigned colb = (unsigned)(cq * 128 + w * 64 + 4 * c) * 2u;
    const unsigned wdb = (unsigned)(pidx * 28 + 8 * g) * 1024u + colb;
#pragma unroll
    for (int i = 0; i < 8; ++i) r2[i] = __builtin_amdgcn_raw_buffer_load_b64(rwd, (8 * g + i < 28) ? wdb + i * 1024 : OOB, 0, W_AUX);
    const unsigned wob = (unsigned)(kvh * 512 + rb * 64 + 8 * g) * 1024u + colb;
#pragma unroll
    for (int i = 0; i < 8; ++i) r0[i] = __builtin_amdgcn_raw_buffer_load_b64(rwo, wob + i * 1024, 0, W_AUX);
#pragma unroll
    for (int i = 0; i < 8; ++i) r1[i] = __builtin_amdgcn_raw_buffer_load_b64(rwo, wob + 32768 + i * 1024, 0, W_AUX);
    SB();
    unsigned fgo = WS_FG + (unsigned)((((g * 16 + cq * 4 + w * 2 + (c >> 3)) * 64 + pidx) * 32 + (c & 7) * 4) * 4);
    lds_wait(L_FLAG2, 1u);
    const v4u hf = *(LAS v4u*)(L + L_H + ((c >> 2) * 32 + 8 * g) * 2);
    tr8x4(r2, bd);
    f32x4 db[4];
#pragma unroll
    for (int e = 0; e < 4; ++e) db[e] = mfma32(hf, bd[e], zero);
    tr8x4(r0, bo[0]); tr8x4(r1, bo[1]);
#pragma unroll
    for (int e = 0; e < 4; ++e) asm volatile("" : "+v"(bo[0][e]), "+v"(bo[1][e]), "+v"(db[e]));
    asm volatile("" : "+v"(fgo));
    STAMP(10);
    lds_wait(L_FLAG2 + 4, 4u);
    STAMP(5);
    const v4u of0 = *(LAS v4u*)(L + L_O + ((c >> 2) * 64 + 8 * g) * 2), of1 = *(LAS v4u*)(L + L_O + ((c >> 2) * 64 + 32 + 8 * g) * 2);
#pragma unroll
    for (int e = 0; e < 4; ++e) db[e] = mfma32(of0, bo[0][e], db[e]);
#pragma unroll
    for (int e = 0; e < 4; ++e) db[e] = mfma32(of1, bo[1][e], db[e]);
    __builtin_amdgcn_raw_buffer_store_b128(v4u{emb(db[0][0], t8), emb(db[1][0], t8), emb(db[2][0], t8), emb(db[3][0], t8)}, rws, fgo, 0, AUX_SC1);
    STAMP(6);
  } else if (w == 2) {
    const unsigned go = c < 7 ? WS_GU + (unsigned)((g * 1792 + pidx * 28 + c * 4) * 4) : OOB;
    v4u xv[4];
#pragma unroll
    for (int b = 0; b < 4; ++b) xv[b] = __builtin_amdgcn_raw_buffer_load_b128(rx, lane * 16 + b * 1024, 0, 0);
    const v4u gv = __builtin_amdgcn_raw_buffer_load_b128(rgw, lane * 16, 0, 0);
    v4u gq[4];
    for (unsigned spin = 0; spin < SPIN_CAP; ++spin) {
      CB();
#pragma unroll
      for (int i = 0; i < 4; ++i) gq[i] = __builtin_amdgcn_raw_buffer_load_b128(rws, go + i * 28672, 0, AUX_SC1);
      SB();
      if (__builtin_amdgcn_ballot_w64(c >= 7 || (emb_bad(gq[0], t8) | emb_bad(gq[1], t8) | emb_bad(gq[2], t8) | emb_bad(gq[3], t8)) == 0u) == ~0ull) break;
      __builtin_amdgcn_s_sleep(2);
    }
    float a[4];
#pragma unroll
    for (int b = 0; b < 4; ++b) { a[b] = dot2(xv[b].x, gv.x, 0.f); a[b] = dot2(xv[b].y, gv.y, a[b]); a[b] = dot2(xv[b].z, gv.z, a[b]); a[b] = dot2(xv[b].w, gv.w, a[b]); }
    a[0] = swap32_add(a[0], a[2]); a[1] = swap32_add(a[1], a[3]);
    a[0] = swap16_add(a[0], a[1]);
    a[0] += dpp<ROW_ROR + 8>(a[0]); a[0] += dpp<ROW_ROR + 4>(a[0]); a[0] += dpp<ROW_ROR + 2>(a[0]); a[0] += dpp<ROW_ROR + 1>(a[0]);
    const float gt = 1.f / (1.f + __expf(-a[0]));
    if (c == 0) *(LAS float*)(L + L_GATE + g * 4) = gt;
    float hh_[4];
#pragma unroll
    for (int i = 0; i < 4; ++i) hh_[i] = (1.f - gt) * (unemb(gq[2][i]) + unemb(gq[3][i])) / (1.f + __expf(-(unemb(gq[0][i]) + unemb(gq[1][i]))));
    if (c < 8) *(LAS v2u*)(L + L_H + (g * 16 + c * 2) * 4) = c < 7 ? v2u{pk_bf16(hh_[0], hh_[1]), pk_bf16(hh_[2], hh_[3])} : v2u{0u, 0u};
    LGKM0();
    if (lane == 0) *(volatile LAS unsigned*)(L + L_FLAG2) = 1u;
  } else if (w >= 4) {
    const int v = w - 4;
    const int sp = lane >> 3;
    unsigned pgo = WS_PG + (unsigned)(((kvh * 32 + r) * 512 + v * 128 + lane * 2) * 4);
    unsigned po = WS_PG + (unsigned)(((kvh * 32 + v * 8 + sp) * 512 + rb * 64 + (lane & 7) * 4) * 4);
    asm volatile("" : "+v"(pgo), "+v"(po));
    lds_wait(L_FLAG + 12, 8u);
    {
      f32x4 ml[4]; f32x2 ov[8];
#pragma unroll
      for (int i = 0; i < 4; ++i) ml[i] = *(LAS f32x4*)(L + L_ML + v * 64 + i * 16);
#pragma unroll
      for (int i = 0; i < 8; ++i) ov[i] = *(LAS f32x2*)(L + L_MRG + (i * 512 + v * 128 + lane * 2) * 4);
      SB();
      const float M = fmaxf(fmaxf(fmaxf(ml[0][0], ml[0][2]), fmaxf(ml[1][0], ml[1][2])), fmaxf(fmaxf(ml[2][0], ml[2][2]), fmaxf(ml[3][0], ml[3][2])));
      float Ls = 0.f; f32x2 o = {0.f, 0.f};
#pragma unroll
      for (int i = 0; i < 4; ++i) {
        const float w0 = __builtin_amdgcn_exp2f(ml[i][0] - M), w1 = __builtin_amdgcn_exp2f(ml[i][2] - M);
        Ls += w0 * ml[i][1] + w1 * ml[i][3]; o += w0 * ov[2 * i] + w1 * ov[2 * i + 1];
      }
      o *= __builtin_amdgcn_rcpf(Ls);
      const unsigned lseu = __float_as_uint(M + __builtin_amdgcn_logf(Ls));
      const unsigned nb2 = lseu >> ((lane & 3) * 8);
      const unsigned t4 = t8 & 0x0fu;
      __builtin_amdgcn_raw_buffer_store_b64(v2u{emb(o.x, t4 | ((nb2 & 15u) << 4)), emb(o.y, t4 | (nb2 & 0xf0u))}, rws, (EXP_HALF && (lane & 16)) ? OOB : pgo, 0, AUX_SC1);
      SB();
    }
    STAMP(4);
    {
      v4u pl0 = {0, 0, 0, 0}, pl1 = {0, 0, 0, 0};
      bool pok = false;
      auto chk1 = [&](const v4u& x, const v4u& y, bool live) -> bool {
        const bool mine = live && ((((x.x ^ t8) | (x.y ^ t8) | (x.z ^ t8) | (x.w ^ t8)) | ((y.x ^ t8) | (y.y ^ t8) | (y.z ^ t8) | (y.w ^ t8))) & 0x0fu) == 0u;
        const unsigned long long bm = __builtin_amdgcn_ballot_w64(mine);
        const bool take = ((unsigned)(bm >> (lane & 56)) & 0xffu) == 0xffu;
#pragma unroll
        for (int i = 0; i < 4; ++i) { pl0[i] = take ? x[i] : pl0[i]; pl1[i] = take ? y[i] : pl1[i]; }
        pok = pok || take;
        return __builtin_amdgcn_ballot_w64(!pok) == 0ull;
      };
      lds_wait(L_FLAG2, 1u);
      const float gate = *(LAS float*)(L + L_GATE + v * 4);
      for (unsigned spin = 0; spin < SPIN_CAP; ++spin) {
        CB();
        const bool lv = !pok;
        const unsigned o_ = lv ? po : OOB;
        const v4u a0 = __builtin_amdgcn_raw_buffer_load_b128(rws, o_, 0, AUX_SC1);
        const v4u a1 = EXP_HALF ? a0 : __builtin_amdgcn_raw_buffer_load_b128(rws, o_ + 128, 0, AUX_SC1);
        SB();
        if (chk1(a0, a1, lv)) break;
      }
      const unsigned half16 = ((pl0.x >> 4) & 15u) | (pl0.y & 0xf0u) | ((pl0.z & 0xf0u) << 4) | ((pl0.w & 0xf0u) << 8);
      const unsigned other = __builtin_amdgcn_mov_dpp(half16, 0xB1, 0xf, 0xf, true);
      const float lse = __uint_as_float((lane & 1) ? (other | (half16 << 16)) : (half16 | (other << 16)));
      float M = fmaxf(lse, dpp<ROW_ROR + 8>(lse)); M = xrow_max(M);
      const float wt_ = __builtin_amdgcn_exp2f(lse - M);
      float W = wt_; W += dpp<ROW_ROR + 8>(W); W = xrow_sum(W);
      float u[4];
#pragma unroll
      for (int i = 0; i < 4; ++i) u[i] = swap32_add(wt_ * unemb(pl0[i]), wt_ * unemb(pl1[i]));
      u[0] = swap16_add(u[0], u[2]); u[1] = swap16_add(u[1], u[3]);
      u[0] += dpp<ROW_ROR + 8>(u[0]); u[1] += dpp<ROW_ROR + 8>(u[1]);
      const float inv = gate * __builtin_amdgcn_rcpf(W);
      if ((lane & 8) == 0) *(LAS unsigned*)(L + L_O + (v * 64 + (lane >> 5) * 32 + (lane & 7) * 4 + ((lane >> 4) & 1) * 2) * 2) = pk_bf16(u[0] * inv, u[1] * inv);
      LGKM0();
      (void)lds_inc(L + L_FLAG2 + 4, lane);
      STAMP(1);
    }
    if ((r & 3) == 0) {
      const int R = (r >> 2) * 8 + kvh;
      const unsigned fo = WS_FG + (unsigned)(R * 8192 + v * 2048 + lane * 16);
      v4u pl0 = {0, 0, 0, 0}, pl1 = {0, 0, 0, 0};
      bool ok0 = false, ok1 = false;
      auto chk2 = [&](const v4u& x, const v4u& y, bool l0, bool l1) -> bool {
        const bool t0_ = l0 && emb_ok(x, t8), t1_ = l1 && emb_ok(y, t8);
#pragma unroll
        for (int i = 0; i < 4; ++i) { pl0[i] = t0_ ? x[i] : pl0[i]; pl1[i] = t1_ ? y[i] : pl1[i]; }
        ok0 = ok0 || t0_; ok1 = ok1 || t1_;
        return __builtin_amdgcn_ballot_w64(!(ok0 && ok1)) == 0ull;
      };
      for (unsigned spin = 0; spin < SPIN_CAP; ++spin) {
        CB();
        const bool l0 = !ok0, l1 = !ok1;
        const v4u c0 = __builtin_amdgcn_raw_buffer_load_b128(rws, l0 ? fo : OOB, 0, AUX_SC1);
        const v4u c1 = __builtin_amdgcn_raw_buffer_load_b128(rws, l1 ? fo + 1024 : OOB, 0, AUX_SC1);
        SB();
        if (chk2(c0, c1, l0, l1)) break;
      }
      float v0 = unemb(pl0.x) + unemb(pl1.x), v1 = unemb(pl0.y) + unemb(pl1.y), v2 = unemb(pl0.z) + unemb(pl1.z), v3 = unemb(pl0.w) + unemb(pl1.w);
      v0 = swap32_add(v0, v2); v1 = swap32_add(v1, v3);
      v0 = swap16_add(v0, v0); v1 = swap16_add(v1, v1);
      v0 += dpp<ROW_ROR + 8>(v0); v1 += dpp<ROW_ROR + 8>(v1);
      if ((lane & 24) == 0) *(LAS f32x2*)(L + L_RED + (v * 32 + (lane & 7) * 4 + (lane >> 5) * 2) * 4) = f32x2{v0, v1};
      LGKM0();
      const unsigned old = lds_inc(L + L_FLAG2 + 8, lane);
      CB();
      if (old == 3 && lane < 16) {
        const LAS f32x2* fr = (const LAS f32x2*)(L + L_RED) + lane;
        const f32x2 s = (fr[0] + fr[16]) + (fr[32] + fr[48]);
        __builtin_amdgcn_raw_buffer_store_b32(pk_bf16(s.x, s.y), mk(pout, 4096), (R * 16 + lane) * 4, 0, 0);
        __builtin_amdgcn_raw_buffer_store_b32(tag, rws, (blk == 0 && lane == 0) ? 0u : OOB, 0, AUX_SC1);
      }
    }
  }
  STAMP(7);
}
}

extern "C" void kernel_launch(void* const* d_in, const int* in_sizes, int n_in, void* d_out, int out_size, void* d_ws,
                              size_t ws_size, hipStream_t stream) {
  fk::fused_kernel<<<256, 512, 0, stream>>>(d_in[0], d_in[1], d_in[2], d_in[3], d_in[4], d_in[6], d_in[7], d_in[8], d_in[9], d_out, d_ws);
}
```

```cpp
#define NBUF 4
#define QPRE 3
#define SLP1 0
#define SLP2 0
#define WSLP 1
#define W_AUX 0
#define RCOND ((blockIdx.x >> 6) == 3 && (blockIdx.x & 7) >= 4)
#define KJM_A 0
#define VJM_A 255
#include <hip/hip_runtime.h>
#include <cstdint>

#define K_AUX 2
#define V_AUX 2
#define DEFER 1
#define PIPE 1
#define GAP1 0
#define GAP2 0
#define DTH 8u
#define EXP_HALF 0
#define KJM 0
#define VJM 0
#define STAMPS 0
#define PREK 4
#define PREV 4
#define EXP_NOTILE 0
#define EXP_NOTW 0
#define EXP_NOX 0
#define EXP_NOQ 0
#define X_AUX 0
#define SPEC 2

typedef unsigned v4u __attribute__((ext_vector_type(4)));
typedef unsigned v2u __attribute__((ext_vector_type(2)));
typedef float f32x4 __attribute__((ext_vector_type(4)));
typedef float f32x2 __attribute__((ext_vector_type(2)));
typedef short s16x4 __attribute__((ext_vector_type(4)));
typedef __bf16 bf16x8 __attribute__((ext_vector_type(8)));
typedef __bf16 bf16x2 __attribute__((ext_vector_type(2)));
typedef __amdgpu_buffer_rsrc_t rsrc_t;
#define LAS __attribute__((address_space(3)))

namespace fk {
constexpr int AUX_NT = 2, AUX_SC1 = 16;
constexpr unsigned OOB = 0x40000000u;
constexpr unsigned WS_QG = 4096;
constexpr unsigned WS_GU = WS_QG + 131072;
constexpr unsigned WS_PG = WS_GU + 114688;
constexpr unsigned WS_FG = WS_PG + 524288;
constexpr unsigned WS_END = WS_FG + 524288;
constexpr unsigned WS_STAMP = 8u << 20;
constexpr unsigned SPIN_CAP = 1u << 17;

constexpr int L_RED = 0;
constexpr int L_Q = 8192;
constexpr int L_H = L_Q + 1024;
constexpr int L_GATE = L_H + 256;
constexpr int L_FLAG = L_GATE + 16;
constexpr int L_FLAG2 = L_FLAG + 16;
constexpr int L_O = L_FLAG2 + 16;
constexpr int L_ML = L_O + 512;
constexpr int L_MRG = L_ML + 256;
constexpr int L_END = L_MRG + 16384;

__device__ __forceinline__ rsrc_t mk(const void* p, unsigned bytes) {
  return __builtin_amdgcn_make_buffer_rsrc((void*)p, 0, (int)bytes, 0x00020000);
}
__device__ __forceinline__ float dot2(unsigned a, unsigned b, float c) {
  return __builtin_amdgcn_fdot2_f32_bf16(__builtin_bit_cast(bf16x2, a), __builtin_bit_cast(bf16x2, b), c, false);
}
__device__ __forceinline__ unsigned pk_bf16(float lo, float hi) {
  f32x2 v = {lo, hi};
  return __builtin_bit_cast(unsigned, __builtin_convertvector(v, bf16x2));
}
template <int CTRL> __device__ __forceinline__ float dpp(float x) {
  return __builtin_bit_cast(float, __builtin_amdgcn_mov_dpp(__builtin_bit_cast(int, x), CTRL, 0xf, 0xf, true));
}
__device__ __forceinline__ float swap32_add(float a, float b) {
  auto s = __builtin_amdgcn_permlane32_swap(__float_as_uint(a), __float_as_uint(b), false, false);
  return __uint_as_float(s[0]) + __uint_as_float(s[1]);
}
__device__ __forceinline__ float swap16_add(float a, float b) {
  auto s = __builtin_amdgcn_permlane16_swap(__float_as_uint(a), __float_as_uint(b), false, false);
  return __uint_as_float(s[0]) + __uint_as_float(s[1]);
}
__device__ __forceinline__ float xrow_max(float x) {
  auto s = __builtin_amdgcn_permlane16_swap(__float_as_uint(x), __float_as_uint(x), false, false);
  x = fmaxf(__uint_as_float(s[0]), __uint_as_float(s[1]));
  auto t = __builtin_amdgcn_permlane32_swap(__float_as_uint(x), __float_as_uint(x), false, false);
  return fmaxf(__uint_as_float(t[0]), __uint_as_float(t[1]));
}
__device__ __forceinline__ float xrow_sum(float x) {
  x = swap16_add(x, x);
  return swap32_add(x, x);
}
constexpr int ROW_SHL = 0x100, ROW_SHR = 0x110, ROW_ROR = 0x120;
__device__ __forceinline__ f32x4 treduce8(float (&v)[32], int lane) {
#pragma unroll
  for (int i = 0; i < 16; ++i) v[i] = swap32_add(v[i], v[i + 16]);
#pragma unroll
  for (int i = 0; i < 8; ++i) v[i] = swap16_add(v[i], v[i + 8]);
#pragma unroll
  for (int i = 0; i < 4; ++i) { float t1 = v[i] + dpp<ROW_ROR + 8>(v[i]), t2 = v[i + 4] + dpp<ROW_ROR + 8>(v[i + 4]); v[i] = (lane & 8) ? t2 : t1; }
  return f32x4{v[0], v[1], v[2], v[3]};
}
template <bool FIRST>
__device__ __forceinline__ void rowpair(float (&v)[32], const v4u& w0, const v4u& w1, unsigned x0, unsigned x1, unsigned x2, unsigned x3) {
  const unsigned xp[4] = {x0, x1, x2, x3};
#pragma unroll
  for (int d = 0; d < 4; ++d) {
    const unsigned lo = __builtin_amdgcn_perm(w1[d], w0[d], 0x05040100u), hi = __builtin_amdgcn_perm(w1[d], w0[d], 0x07060302u);
#pragma unroll
    for (int b = 0; b < 4; ++b) {
      v[b * 8 + 2 * d] = dot2(lo, xp[b], FIRST ? 0.f : v[b * 8 + 2 * d]);
      v[b * 8 + 2 * d + 1] = dot2(hi, xp[b], FIRST ? 0.f : v[b * 8 + 2 * d + 1]);
    }
  }
}
__device__ __forceinline__ f32x4 mfma32(v4u a, v4u b, f32x4 c) {
  return __builtin_amdgcn_mfma_f32_16x16x32_bf16(__builtin_bit_cast(bf16x8, a), __builtin_bit_cast(bf16x8, b), c, 0, 0, 0);
}
__device__ __forceinline__ f32x4 mfma16(v2u a, v2u b, f32x4 c) {
  return __builtin_amdgcn_mfma_f32_16x16x16bf16_1k(__builtin_bit_cast(s16x4, a), __builtin_bit_cast(s16x4, b), c, 0, 0, 0);
}
__device__ __forceinline__ float sel4(const f32x4& d, int g) { return g == 0 ? d[0] : g == 1 ? d[1] : g == 2 ? d[2] : d[3]; }
__device__ __forceinline__ unsigned lds_inc(LAS unsigned char* p, int lane) {
  unsigned old = 0;
  if (lane == 0) old = __hip_atomic_fetch_add((LAS unsigned*)p, 1u, __ATOMIC_RELAXED, __HIP_MEMORY_SCOPE_WORKGROUP);
  return __builtin_amdgcn_readfirstlane(old);
}
__device__ __forceinline__ unsigned emb(float v, unsigned t8) { return ((__float_as_uint(v) + 0x80u) & 0xffffff00u) | t8; }
__device__ __forceinline__ bool emb_ok(const v4u& a, unsigned t8) { return (((a.x ^ t8) | (a.y ^ t8) | (a.z ^ t8) | (a.w ^ t8)) & 0xffu) == 0u; }
__device__ __forceinline__ unsigned emb_bad(const v4u& a, unsigned t8) { return ((a.x ^ t8) | (a.y ^ t8) | (a.z ^ t8) | (a.w ^ t8)) & 0xffu; }
__device__ __forceinline__ float unemb(unsigned u) { return __uint_as_float(u & 0xffffff00u); }
__device__ __forceinline__ void tr8x4(const v2u (&r)[8], v4u (&f)[4]) {
#pragma unroll
  for (int i = 0; i < 4; ++i) {
    f[0][i] = __builtin_amdgcn_perm(r[2 * i + 1].x, r[2 * i].x, 0x05040100u); f[1][i] = __builtin_amdgcn_perm(r[2 * i + 1].x, r[2 * i].x, 0x07060302u);
    f[2][i] = __builtin_amdgcn_perm(r[2 * i + 1].y, r[2 * i].y, 0x05040100u); f[3][i] = __builtin_amdgcn_perm(r[2 * i + 1].y, r[2 * i].y, 0x07060302u);
  }
}
#define SB() __builtin_amdgcn_sched_barrier(0)
#define CB() asm volatile("" ::: "memory")
#define LGKM0() asm volatile("s_waitcnt lgkmcnt(0)" ::: "memory")
#define BAR() do { LGKM0(); __builtin_amdgcn_s_barrier(); CB(); } while (0)
#define STAMP(i) do { } while (0)

__global__ void __launch_bounds__(512, 2)
fused_kernel(const void* __restrict__ px, const void* __restrict__ pk, const void* __restrict__ pv, const void* __restrict__ pwq,
             const void* __restrict__ pwo, const void* __restrict__ pwg, const void* __restrict__ pwu, const void* __restrict__ pwd,
             const void* __restrict__ pgw, void* __restrict__ pout, void* pws) {
  __shared__ __attribute__((aligned(16))) unsigned char smem[L_END];
  LAS unsigned char* L = (LAS unsigned char*)smem;
  if (RCOND) {
  STAMP(0);
  asm volatile("" :: "s"(px), "s"(pk), "s"(pv), "s"(pwq), "s"(pwo), "s"(pwg), "s"(pwu), "s"(pwd), "s"(pgw), "s"(pout), "s"(pws));
  const int tid = threadIdx.x, lane = tid & 63, w = __builtin_amdgcn_readfirstlane(tid >> 6);
  const int g = lane >> 4, c = lane & 15;
  const int blk = blockIdx.x, kvh = blk & 7, r = blk >> 3;
  const int b_att = r >> 3, s_att = r & 7;
  const int rb = r >> 2, cq = r & 3;
  const int pidx = kvh * 8 + rb;

  const size_t kvoff = ((size_t)(b_att * 8 + kvh) * 8192 + s_att * 1024) * 256;
  const rsrc_t rk = mk((const char*)pk + kvoff, 262144), rv = mk((const char*)pv + kvoff, 262144);
  const rsrc_t rws = mk(pws, WS_END);
  const rsrc_t rx = mk(px, 4096), rgw = mk(pgw, 1024);
  const rsrc_t rwo = mk(pwo, 4096 * 512 * 2), rwd = mk(pwd, 1792 * 512 * 2);

  __builtin_amdgcn_s_setprio(3);
  const bool isq = r < 16;
  const int gu = kvh * 14 + (r - 16);
  const int um = gu >= 56 ? 1 : 0;
  const int ulc = isq ? kvh * 8 + (r >> 1) : ((gu - 56 * um) >> 1);
  const int uh = isq ? (r & 1) : (gu & 1);
  v4u wt[4]; v2u xq[4];
  {
    const int row = uh * 256 + w * 32 + 4 * (lane >> 3);
    const unsigned rstr = isq ? 8192u : 3584u;
    const rsrc_t ru = mk(isq ? pwq : (um ? pwu : pwg), isq ? 512 * 4096 * 2 : 512 * 1792 * 2);
    const unsigned uo = r < 30 ? (unsigned)row * rstr + (unsigned)(ulc * 128 + (lane & 7) * 16) : OOB;
#pragma unroll
    for (int i = 0; i < 4; ++i) wt[i] = __builtin_amdgcn_raw_buffer_load_b128(ru, uo + i * rstr, 0, W_AUX);
#pragma unroll
    for (int b = 0; b < 4; ++b) xq[b] = __builtin_amdgcn_raw_buffer_load_b64(rx, row * 2 + b * 1024, 0, X_AUX);
  }
  SB();
  if (tid == 0) { *(volatile LAS v4u*)(L + L_FLAG) = v4u{0u, 0u, 0u, 0u}; *(volatile LAS v4u*)(L + L_FLAG2) = v4u{0u, 0u, 0u, 0u}; }
  BAR();
  SB();
  const unsigned ko = c * 256 + g * 16, vo = g * 1024 + c * 16;
  v4u kb[NBUF][4], vb[NBUF][4];
  auto issueK = [&](int j, int i0, int i1) {
    const unsigned so = (unsigned)(j * 8 + w) * 4096u;
#pragma unroll
    for (int i = i0; i < i1; ++i) kb[j % NBUF][i] = ((KJM_A >> j) & 1) ? __builtin_amdgcn_raw_buffer_load_b128(rk, ko + i * 64, so, 0) : __builtin_amdgcn_raw_buffer_load_b128(rk, ko + i * 64, so, 2);
    SB();
  };
  auto issueV = [&](int j, int i0, int i1) {
    const unsigned so = (unsigned)(j * 8 + w) * 4096u;
#pragma unroll
    for (int i = i0; i < i1; ++i) vb[j % NBUF][i] = ((VJM_A >> j) & 1) ? __builtin_amdgcn_raw_buffer_load_b128(rv, vo + i * 256, so, 0) : __builtin_amdgcn_raw_buffer_load_b128(rv, vo + i * 256, so, 2);
    SB();
  };
  auto issue = [&](int j) { issueK(j, 0, 4); issueV(j, 0, 4); STAMP(11 + j); };
  const unsigned E = *(const __attribute__((address_space(4))) unsigned*)pws;
  SB();
  issueV(0, 0, PREV);
  issueK(0, 0, PREK);
  const unsigned tag = E * 0x9E3779B5u + 0x7F4A7C15u;
  const unsigned t8 = tag & 0xffu;
  {
    LAS f32x4* red = (LAS f32x4*)(L + L_RED);
    float v[32];
    rowpair<true>(v, wt[0], wt[1], xq[0].x, xq[1].x, xq[2].x, xq[3].x);
    rowpair<false>(v, wt[2], wt[3], xq[0].y, xq[1].y, xq[2].y, xq[3].y);
    red[w * 64 + lane] = treduce8(v, lane);
    LGKM0();
    const unsigned old = lds_inc(L + L_FLAG + 4, lane);
    CB();
    const f32x4 s = ((red[lane] + red[64 + lane]) + (red[128 + lane] + red[192 + lane])) + ((red[256 + lane] + red[320 + lane]) + (red[384 + lane] + red[448 + lane]));
    const int c64 = (lane & 7) * 8 + ((lane >> 3) & 1) * 4;
    const unsigned po = isq ? WS_QG + (unsigned)((((kvh * 4 + g) * 2 + uh) * 512 + (r >> 1) * 64 + c64) * 4)
                            : WS_GU + (unsigned)((((um * 2 + uh) * 4 + g) * 1792 + ulc * 64 + c64) * 4);
    __builtin_amdgcn_raw_buffer_store_b128(v4u{emb(s[0], t8), emb(s[1], t8), emb(s[2], t8), emb(s[3], t8)}, rws, (old == 7 && r < 30) ? po : OOB, 0, AUX_SC1);
    SB();
    __builtin_amdgcn_s_setprio(0);
    SB();
  }
  issueV(0, PREV, 4);
  issueK(0, PREK, 4);
  STAMP(11);
#pragma unroll
  for (int j = 1; j < QPRE; ++j) issue(j);
  const unsigned qgo = w < 2 ? WS_QG + (unsigned)((kvh * 4 + b_att) * 4096 + w * 1024 + lane * 16) : OOB;
  CB();
  v4u qa = __builtin_amdgcn_raw_buffer_load_b128(rws, qgo, 0, AUX_SC1);
  v4u qb = __builtin_amdgcn_raw_buffer_load_b128(rws, qgo + 2048, 0, AUX_SC1);
  SB();
  issue(QPRE);
  if (w < 2) {
    if (__builtin_amdgcn_ballot_w64((emb_bad(qa, t8) | emb_bad(qb, t8)) != 0u) != 0ull) {
      for (unsigned spin = 0; spin < SPIN_CAP; ++spin) {
        CB();
        qa = __builtin_amdgcn_raw_buffer_load_b128(rws, qgo, 0, AUX_SC1);
        qb = __builtin_amdgcn_raw_buffer_load_b128(rws, qgo + 2048, 0, AUX_SC1);
        if (__builtin_amdgcn_ballot_w64((emb_bad(qa, t8) | emb_bad(qb, t8)) != 0u) == 0ull) break;
      }
    }
    *(LAS v2u*)(L + L_Q + (w * 128 + lane * 2) * 4) = v2u{pk_bf16(unemb(qa.x) + unemb(qb.x), unemb(qa.y) + unemb(qb.y)), pk_bf16(unemb(qa.z) + unemb(qb.z), unemb(qa.w) + unemb(qb.w))};
    LGKM0();
    (void)lds_inc(L + L_FLAG, lane);
    STAMP(9);
  }
  SB();
#pragma unroll
  for (int j = QPRE + 1; j < NBUF; ++j) issue(j);
  for (unsigned spin = 0; spin < (SPIN_CAP << 4); ++spin) {
    if (*(volatile LAS unsigned*)(L + L_FLAG) >= 2u) break;
    __builtin_amdgcn_s_sleep(1);
  }
  CB();
  STAMP(2);
  v4u qf[4];
#pragma unroll
  for (int i = 0; i < 4; ++i) qf[i] = *(LAS v4u*)(L + L_Q + (((c & 3) * 64 + 16 * i + 4 * g) * 4));

  constexpr float C2 = 0.08838834764831845f * 1.4426950408889634f;
  f32x4 acc[8];
#pragma unroll
  for (int e = 0; e < 8; ++e) acc[e] = f32x4{0.f, 0.f, 0.f, 0.f};
  float m = -INFINITY, l = 0.f;
#pragma unroll
  for (int j = 0; j < 8; ++j) {
    const int bf = j % NBUF;
    f32x4 sc = {0.f, 0.f, 0.f, 0.f};
#pragma unroll
    for (int i = 0; i < 4; ++i) sc = mfma32(kb[bf][i], qf[i], sc);
    const float t0 = sc[0] * C2, t1 = sc[1] * C2, t2 = sc[2] * C2, t3 = sc[3] * C2;
    const float cm = xrow_max(fmaxf(fmaxf(t0, t1), fmaxf(t2, t3)));
    const float mn = fmaxf(m, cm);
    const float alpha = __builtin_amdgcn_exp2f(m - mn);
    const float p0 = __builtin_amdgcn_exp2f(t0 - mn), p1 = __builtin_amdgcn_exp2f(t1 - mn), p2 = __builtin_amdgcn_exp2f(t2 - mn), p3 = __builtin_amdgcn_exp2f(t3 - mn);
    l = l * alpha + ((p0 + p1) + (p2 + p3));
    m = mn;
    const v2u pa = {pk_bf16(p0, p1), pk_bf16(p2, p3)};
    if (j > 0) {
      const float a0 = dpp<0x00>(alpha), a1 = dpp<0x55>(alpha), a2 = dpp<0xAA>(alpha), a3 = dpp<0xFF>(alpha);
#pragma unroll
      for (int e = 0; e < 8; ++e) { acc[e][0] *= a0; acc[e][1] *= a1; acc[e][2] *= a2; acc[e][3] *= a3; }
    }
#pragma unroll
    for (int d = 0; d < 4; ++d) {
      const v2u lo = {__builtin_amdgcn_perm(vb[bf][1][d], vb[bf][0][d], 0x05040100u), __builtin_amdgcn_perm(vb[bf][3][d], vb[bf][2][d], 0x05040100u)};
      const v2u hi = {__builtin_amdgcn_perm(vb[bf][1][d], vb[bf][0][d], 0x07060302u), __builtin_amdgcn_perm(vb[bf][3][d], vb[bf][2][d], 0x07060302u)};
      acc[2 * d] = mfma16(pa, lo, acc[2 * d]);
      acc[2 * d + 1] = mfma16(pa, hi, acc[2 * d + 1]);
    }
    if (j == QPRE) asm volatile("" :: "v"(qa), "v"(qb));
    SB();
    if (j + NBUF < 8) issue(j + NBUF);
    if (j + NBUF == 7) { if (lane == 0) (void)__hip_atomic_fetch_add((LAS unsigned*)(L + L_FLAG + 8), 1u, __ATOMIC_RELAXED, __HIP_MEMORY_SCOPE_WORKGROUP); SB(); }
  }
  STAMP(3);
  l = xrow_sum(l);
  if (g == 0) {
    LAS float* mo = (LAS float*)(L + L_MRG) + w * 512 + 8 * c;
#pragma unroll
    for (int h = 0; h < 4; ++h) {
      *(LAS f32x4*)(mo + h * 128) = f32x4{acc[0][h], acc[1][h], acc[2][h], acc[3][h]};
      *(LAS f32x4*)(mo + h * 128 + 4) = f32x4{acc[4][h], acc[5][h], acc[6][h], acc[7][h]};
    }
    if (c < 4) *(LAS f32x2*)(L + L_ML + (c * 8 + w) * 8) = f32x2{m, l};
  }
  LGKM0();
  (void)lds_inc(L + L_FLAG + 12, lane);
  auto lds_wait = [&](int off, unsigned n) {
    for (unsigned spin = 0; spin < (SPIN_CAP << 4); ++spin) {
      if (*(volatile LAS unsigned*)(L + off) >= n) break;
      __builtin_amdgcn_s_sleep(WSLP);
    }
    CB();
  };
  const f32x4 zero = {0.f, 0.f, 0.f, 0.f};
  if (w < 2) {
    for (unsigned spin = 0; spin < (SPIN_CAP << 4); ++spin) {
      if (*(volatile LAS unsigned*)(L + L_FLAG + 8) >= DTH) break;
      __builtin_amdgcn_s_sleep(1);
    }
    CB();
    v2u r0[8], r1[8], r2[8];
    v4u bo[2][4], bd[4];
    const unsigned colb = (unsigned)(cq * 128 + w * 64 + 4 * c) * 2u;
    const unsigned wdb = (unsigned)(pidx * 28 + 8 * g) * 1024u + colb;
#pragma unroll
    for (int i = 0; i < 8; ++i) r2[i] = __builtin_amdgcn_raw_buffer_load_b64(rwd, (8 * g + i < 28) ? wdb + i * 1024 : OOB, 0, W_AUX);
    const unsigned wob = (unsigned)(kvh * 512 + rb * 64 + 8 * g) * 1024u + colb;
#pragma unroll
    for (int i = 0; i < 8; ++i) r0[i] = __builtin_amdgcn_raw_buffer_load_b64(rwo, wob + i * 1024, 0, W_AUX);
#pragma unroll
    for (int i = 0; i < 8; ++i) r1[i] = __builtin_amdgcn_raw_buffer_load_b64(rwo, wob + 32768 + i * 1024, 0, W_AUX);
    SB();
    unsigned fgo = WS_FG + (unsigned)((((g * 16 + cq * 4 + w * 2 + (c >> 3)) * 64 + pidx) * 32 + (c & 7) * 4) * 4);
    lds_wait(L_FLAG2, 1u);
    const v4u hf = *(LAS v4u*)(L + L_H + ((c >> 2) * 32 + 8 * g) * 2);
    tr8x4(r2, bd);
    f32x4 db[4];
#pragma unroll
    for (int e = 0; e < 4; ++e) db[e] = mfma32(hf, bd[e], zero);
    tr8x4(r0, bo[0]); tr8x4(r1, bo[1]);
#pragma unroll
    for (int e = 0; e < 4; ++e) asm volatile("" : "+v"(bo[0][e]), "+v"(bo[1][e]), "+v"(db[e]));
    asm volatile("" : "+v"(fgo));
    STAMP(10);
    lds_wait(L_FLAG2 + 4, 4u);
    STAMP(5);
    const v4u of0 = *(LAS v4u*)(L + L_O + ((c >> 2) * 64 + 8 * g) * 2), of1 = *(LAS v4u*)(L + L_O + ((c >> 2) * 64 + 32 + 8 * g) * 2);
#pragma unroll
    for (int e = 0; e < 4; ++e) db[e] = mfma32(of0, bo[0][e], db[e]);
#pragma unroll
    for (int e = 0; e < 4; ++e) db[e] = mfma32(of1, bo[1][e], db[e]);
    __builtin_amdgcn_raw_buffer_store_b128(v4u{emb(db[0][0], t8), emb(db[1][0], t8), emb(db[2][0], t8), emb(db[3][0], t8)}, rws, fgo, 0, AUX_SC1);
    STAMP(6);
  } else if (w == 2) {
    const unsigned go = c < 7 ? WS_GU + (unsigned)((g * 1792 + pidx * 28 + c * 4) * 4) : OOB;
    v4u xv[4];
#pragma unroll
    for (int b = 0; b < 4; ++b) xv[b] = __builtin_amdgcn_raw_buffer_load_b128(rx, lane * 16 + b * 1024, 0, 0);
    const v4u gv = __builtin_amdgcn_raw_buffer_load_b128(rgw, lane * 16, 0, 0);
    v4u gq[4];
    for (unsigned spin = 0; spin < SPIN_CAP; ++spin) {
      CB();
#pragma unroll
      for (int i = 0; i < 4; ++i) gq[i] = __builtin_amdgcn_raw_buffer_load_b128(rws, go + i * 28672, 0, AUX_SC1);
      SB();
      if (__builtin_amdgcn_ballot_w64(c >= 7 || (emb_bad(gq[0], t8) | emb_bad(gq[1], t8) | emb_bad(gq[2], t8) | emb_bad(gq[3], t8)) == 0u) == ~0ull) break;
      __builtin_amdgcn_s_sleep(2);
    }
    float a[4];
#pragma unroll
    for (int b = 0; b < 4; ++b) { a[b] = dot2(xv[b].x, gv.x, 0.f); a[b] = dot2(xv[b].y, gv.y, a[b]); a[b] = dot2(xv[b].z, gv.z, a[b]); a[b] = dot2(xv[b].w, gv.w, a[b]); }
    a[0] = swap32_add(a[0], a[2]); a[1] = swap32_add(a[1], a[3]);
    a[0] = swap16_add(a[0], a[1]);
    a[0] += dpp<ROW_ROR + 8>(a[0]); a[0] += dpp<ROW_ROR + 4>(a[0]); a[0] += dpp<ROW_ROR + 2>(a[0]); a[0] += dpp<ROW_ROR + 1>(a[0]);
    const float gt = 1.f / (1.f + __expf(-a[0]));
    if (c == 0) *(LAS float*)(L + L_GATE + g * 4) = gt;
    float hh_[4];
#pragma unroll
    for (int i = 0; i < 4; ++i) hh_[i] = (1.f - gt) * (unemb(gq[2][i]) + unemb(gq[3][i])) / (1.f + __expf(-(unemb(gq[0][i]) + unemb(gq[1][i]))));
    if (c < 8) *(LAS v2u*)(L + L_H + (g * 16 + c * 2) * 4) = c < 7 ? v2u{pk_bf16(hh_[0], hh_[1]), pk_bf16(hh_[2], hh_[3])} : v2u{0u, 0u};
    LGKM0();
    if (lane == 0) *(volatile LAS unsigned*)(L + L_FLAG2) = 1u;
  } else if (w >= 4) {
    const int v = w - 4;
    const int sp = lane >> 3;
    unsigned pgo = WS_PG + (unsigned)(((kvh * 32 + r) * 512 + v * 128 + lane * 2) * 4);
    unsigned po = WS_PG + (unsigned)(((kvh * 32 + v * 8 + sp) * 512 + rb * 64 + (lane & 7) * 4) * 4);
    asm volatile("" : "+v"(pgo), "+v"(po));
    lds_wait(L_FLAG + 12, 8u);
    {
      f32x4 ml[4]; f32x2 ov[8];
#pragma unroll
      for (int i = 0; i < 4; ++i) ml[i] = *(LAS f32x4*)(L + L_ML + v * 64 + i * 16);
#pragma unroll
      for (int i = 0; i < 8; ++i) ov[i] = *(LAS f32x2*)(L + L_MRG + (i * 512 + v * 128 + lane * 2) * 4);
      SB();
      const float M = fmaxf(fmaxf(fmaxf(ml[0][0], ml[0][2]), fmaxf(ml[1][0], ml[1][2])), fmaxf(fmaxf(ml[2][0], ml[2][2]), fmaxf(ml[3][0], ml[3][2])));
      float Ls = 0.f; f32x2 o = {0.f, 0.f};
#pragma unroll
      for (int i = 0; i < 4; ++i) {
        const float w0 = __builtin_amdgcn_exp2f(ml[i][0] - M), w1 = __builtin_amdgcn_exp2f(ml[i][2] - M);
        Ls += w0 * ml[i][1] + w1 * ml[i][3]; o += w0 * ov[2 * i] + w1 * ov[2 * i + 1];
      }
      o *= __builtin_amdgcn_rcpf(Ls);
      const unsigned lseu = __float_as_uint(M + __builtin_amdgcn_logf(Ls));
      const unsigned nb2 = lseu >> ((lane & 3) * 8);
      const unsigned t4 = t8 & 0x0fu;
      __builtin_amdgcn_raw_buffer_store_b64(v2u{emb(o.x, t4 | ((nb2 & 15u) << 4)), emb(o.y, t4 | (nb2 & 0xf0u))}, rws, (EXP_HALF && (lane & 16)) ? OOB : pgo, 0, AUX_SC1);
      SB();
    }
    STAMP(4);
    {
      v4u pl0 = {0, 0, 0, 0}, pl1 = {0, 0, 0, 0};
      bool pok = false;
      auto chk1 = [&](const v4u& x, const v4u& y, bool live) -> bool {
        const bool mine = live && ((((x.x ^ t8) | (x.y ^ t8) | (x.z ^ t8) | (x.w ^ t8)) | ((y.x ^ t8) | (y.y ^ t8) | (y.z ^ t8) | (y.w ^ t8))) & 0x0fu) == 0u;
        const unsigned long long bm = __builtin_amdgcn_ballot_w64(mine);
        const bool take = ((unsigned)(bm >> (lane & 56)) & 0xffu) == 0xffu;
#pragma unroll
        for (int i = 0; i < 4; ++i) { pl0[i] = take ? x[i] : pl0[i]; pl1[i] = take ? y[i] : pl1[i]; }
        pok = pok || take;
        return __builtin_amdgcn_ballot_w64(!pok) == 0ull;
      };
      lds_wait(L_FLAG2, 1u);
      const float gate = *(LAS float*)(L + L_GATE + v * 4);
      for (unsigned spin = 0; spin < SPIN_CAP; ++spin) {
        CB();
        const bool lv = !pok;
        const unsigned o_ = lv ? po : OOB;
        const v4u a0 = __builtin_amdgcn_raw_buffer_load_b128(rws, o_, 0, AUX_SC1);
        const v4u a1 = EXP_HALF ? a0 : __builtin_amdgcn_raw_buffer_load_b128(rws, o_ + 128, 0, AUX_SC1);
        SB();
        if (chk1(a0, a1, lv)) break;
      }
      const unsigned half16 = ((pl0.x >> 4) & 15u) | (pl0.y & 0xf0u) | ((pl0.z & 0xf0u) << 4) | ((pl0.w & 0xf0u) << 8);
      const unsigned other = __builtin_amdgcn_mov_dpp(half16, 0xB1, 0xf, 0xf, true);
      const float lse = __uint_as_float((lane & 1) ? (other | (half16 << 16)) : (half16 | (other << 16)));
      float M = fmaxf(lse, dpp<ROW_ROR + 8>(lse)); M = xrow_max(M);
      const float wt_ = __builtin_amdgcn_exp2f(lse - M);
      float W = wt_; W += dpp<ROW_ROR + 8>(W); W = xrow_sum(W);
      float u[4];
#pragma unroll
      for (int i = 0; i < 4; ++i) u[i] = swap32_add(wt_ * unemb(pl0[i]), wt_ * unemb(pl1[i]));
      u[0] = swap16_add(u[0], u[2]); u[1] = swap16_add(u[1], u[3]);
      u[0] += dpp<ROW_ROR + 8>(u[0]); u[1] += dpp<ROW_ROR + 8>(u[1]);
      const float inv = gate * __builtin_amdgcn_rcpf(W);
      if ((lane & 8) == 0) *(LAS unsigned*)(L + L_O + (v * 64 + (lane >> 5) * 32 + (lane & 7) * 4 + ((lane >> 4) & 1) * 2) * 2) = pk_bf16(u[0] * inv, u[1] * inv);
      LGKM0();
      (void)lds_inc(L + L_FLAG2 + 4, lane);
      STAMP(1);
    }
    if ((r & 3) == 0) {
      const int R = (r >> 2) * 8 + kvh;
      const unsigned fo = WS_FG + (unsigned)(R * 8192 + v * 2048 + lane * 16);
      v4u pl0 = {0, 0, 0, 0}, pl1 = {0, 0, 0, 0};
      bool ok0 = false, ok1 = false;
      auto chk2 = [&](const v4u& x, const v4u& y, bool l0, bool l1) -> bool {
        const bool t0_ = l0 && emb_ok(x, t8), t1_ = l1 && emb_ok(y, t8);
#pragma unroll
        for (int i = 0; i < 4; ++i) { pl0[i] = t0_ ? x[i] : pl0[i]; pl1[i] = t1_ ? y[i] : pl1[i]; }
        ok0 = ok0 || t0_; ok1 = ok1 || t1_;
        return __builtin_amdgcn_ballot_w64(!(ok0 && ok1)) == 0ull;
      };
      for (unsigned spin = 0; spin < SPIN_CAP; ++spin) {
        CB();
        const bool l0 = !ok0, l1 = !ok1;
        const v4u c0 = __builtin_amdgcn_raw_buffer_load_b128(rws, l0 ? fo : OOB, 0, AUX_SC1);
        const v4u c1 = __builtin_amdgcn_raw_buffer_load_b128(rws, l1 ? fo + 1024 : OOB, 0, AUX_SC1);
        SB();
        if (chk2(c0, c1, l0, l1)) break;
      }
      float v0 = unemb(pl0.x) + unemb(pl1.x), v1 = unemb(pl0.y) + unemb(pl1.y), v2 = unemb(pl0.z) + unemb(pl1.z), v3 = unemb(pl0.w) + unemb(pl1.w);
      v0 = swap32_add(v0, v2); v1 = swap32_add(v1, v3);
      v0 = swap16_add(v0, v0); v1 = swap16_add(v1, v1);
      v0 += dpp<ROW_ROR + 8>(v0); v1 += dpp<ROW_ROR + 8>(v1);
      if ((lane & 24) == 0) *(LAS f32x2*)(L + L_RED + (v * 32 + (lane & 7) * 4 + (lane >> 5) * 2) * 4) = f32x2{v0, v1};
      LGKM0();
      const unsigned old = lds_inc(L + L_FLAG2 + 8, lane);
      CB();
      if (old == 3 && lane < 16) {
        const LAS f32x2* fr = (const LAS f32x2*)(L + L_RED) + lane;
        const f32x2 s = (fr[0] + fr[16]) + (fr[32] + fr[48]);
        __builtin_amdgcn_raw_buffer_store_b32(pk_bf16(s.x, s.y), mk(pout, 4096), (R * 16 + lane) * 4, 0, 0);
        __builtin_amdgcn_raw_buffer_store_b32(tag, rws, (blk == 0 && lane == 0) ? 0u : OOB, 0, AUX_SC1);
      }
    }
  }
  STAMP(7);
  __builtin_amdgcn_endpgm();
  }
  {
  STAMP(0);
  asm volatile("" :: "s"(px), "s"(pk), "s"(pv), "s"(pwq), "s"(pwo), "s"(pwg), "s"(pwu), "s"(pwd), "s"(pgw), "s"(pout), "s"(pws));
  const int tid = threadIdx.x, lane = tid & 63, w = __builtin_amdgcn_readfirstlane(tid >> 6);
  const int g = lane >> 4, c = lane & 15;
  const int blk = blockIdx.x, kvh = blk & 7, r = blk >> 3;
  const int b_att = r >> 3, s_att = r & 7;
  const int rb = r >> 2, cq = r & 3;
  const int pidx = kvh * 8 + rb;

  const size_t kvoff = ((size_t)(b_att * 8 + kvh) * 8192 + s_att * 1024) * 256;
  const rsrc_t rk = mk((const char*)pk + kvoff, 262144), rv = mk((const char*)pv + kvoff, 262144);
  const rsrc_t rws = mk(pws, WS_END);
  const rsrc_t rx = mk(px, 4096), rgw = mk(pgw, 1024);
  const rsrc_t rwo = mk(pwo, 4096 * 512 * 2), rwd = mk(pwd, 1792 * 512 * 2);

  __builtin_amdgcn_s_setprio(3);
  const bool isq = r < 16;
  const int gu = kvh * 14 + (r - 16);
  const int um = gu >= 56 ? 1 : 0;
  const int ulc = isq ? kvh * 8 + (r >> 1) : ((gu - 56 * um) >> 1);
  const int uh = isq ? (r & 1) : (gu & 1);
  v4u wt[4]; v2u xq[4];
  {
    const int row = uh * 256 + w * 32 + 4 * (lane >> 3);
    const unsigned rstr = isq ? 8192u : 3584u;
    const rsrc_t ru = mk(isq ? pwq : (um ? pwu : pwg), isq ? 512 * 4096 * 2 : 512 * 1792 * 2);
    const unsigned uo = r < 30 ? (unsigned)row * rstr + (unsigned)(ulc * 128 + (lane & 7) * 16) : OOB;
#pragma unroll
    for (int i = 0; i < 4; ++i) wt[i] = __builtin_amdgcn_raw_buffer_load_b128(ru, uo + i * rstr, 0, W_AUX);
#pragma unroll
    for (int b = 0; b < 4; ++b) xq[b] = __builtin_amdgcn_raw_buffer_load_b64(rx, row * 2 + b * 1024, 0, X_AUX);
  }
  SB();
  if (tid == 0) { *(volatile LAS v4u*)(L + L_FLAG) = v4u{0u, 0u, 0u, 0u}; *(volatile LAS v4u*)(L + L_FLAG2) = v4u{0u, 0u, 0u, 0u}; }
  BAR();
  SB();
  const unsigned ko = c * 256 + g * 16, vo = g * 1024 + c * 16;
  v4u kb[NBUF][4], vb[NBUF][4];
  auto issueK = [&](int j, int i0, int i1) {
    const unsigned so = (unsigned)(j * 8 + w) * 4096u;
#pragma unroll
    for (int i = i0; i < i1; ++i) kb[j % NBUF][i] = ((KJM >> j) & 1) ? __builtin_amdgcn_raw_buffer_load_b128(rk, ko + i * 64, so, 0) : __builtin_amdgcn_raw_buffer_load_b128(rk, ko + i * 64, so, 2);
    SB();
  };
  auto issueV = [&](int j, int i0, int i1) {
    const unsigned so = (unsigned)(j * 8 + w) * 4096u;
#pragma unroll
    for (int i = i0; i < i1; ++i) vb[j % NBUF][i] = ((VJM >> j) & 1) ? __builtin_amdgcn_raw_buffer_load_b128(rv, vo + i * 256, so, 0) : __builtin_amdgcn_raw_buffer_load_b128(rv, vo + i * 256, so, 2);
    SB();
  };
  auto issue = [&](int j) { issueK(j, 0, 4); issueV(j, 0, 4); STAMP(11 + j); };
  const unsigned E = *(const __attribute__((address_space(4))) unsigned*)pws;
  SB();
  issueV(0, 0, PREV);
  issueK(0, 0, PREK);
  const unsigned tag = E * 0x9E3779B5u + 0x7F4A7C15u;
  const unsigned t8 = tag & 0xffu;
  {
    LAS f32x4* red = (LAS f32x4*)(L + L_RED);
    float v[32];
    rowpair<true>(v, wt[0], wt[1], xq[0].x, xq[1].x, xq[2].x, xq[3].x);
    rowpair<false>(v, wt[2], wt[3], xq[0].y, xq[1].y, xq[2].y, xq[3].y);
    red[w * 64 + lane] = treduce8(v, lane);
    LGKM0();
    const unsigned old = lds_inc(L + L_FLAG + 4, lane);
    CB();
    const f32x4 s = ((red[lane] + red[64 + lane]) + (red[128 + lane] + red[192 + lane])) + ((red[256 + lane] + red[320 + lane]) + (red[384 + lane] + red[448 + lane]));
    const int c64 = (lane & 7) * 8 + ((lane >> 3) & 1) * 4;
    const unsigned po = isq ? WS_QG + (unsigned)((((kvh * 4 + g) * 2 + uh) * 512 + (r >> 1) * 64 + c64) * 4)
                            : WS_GU + (unsigned)((((um * 2 + uh) * 4 + g) * 1792 + ulc * 64 + c64) * 4);
    __builtin_amdgcn_raw_buffer_store_b128(v4u{emb(s[0], t8), emb(s[1], t8), emb(s[2], t8), emb(s[3], t8)}, rws, (old == 7 && r < 30) ? po : OOB, 0, AUX_SC1);
    SB();
    __builtin_amdgcn_s_setprio(0);
    SB();
  }
  issueV(0, PREV, 4);
  issueK(0, PREK, 4);
  STAMP(11);
#pragma unroll
  for (int j = 1; j < QPRE; ++j) issue(j);
  const unsigned qgo = w < 2 ? WS_QG + (unsigned)((kvh * 4 + b_att) * 4096 + w * 1024 + lane * 16) : OOB;
  CB();
  v4u qa = __builtin_amdgcn_raw_buffer_load_b128(rws, qgo, 0, AUX_SC1);
  v4u qb = __builtin_amdgcn_raw_buffer_load_b128(rws, qgo + 2048, 0, AUX_SC1);
  SB();
  issue(QPRE);
  if (w < 2) {
    if (__builtin_amdgcn_ballot_w64((emb_bad(qa, t8) | emb_bad(qb, t8)) != 0u) != 0ull) {
      for (unsigned spin = 0; spin < SPIN_CAP; ++spin) {
        CB();
        qa = __builtin_amdgcn_raw_buffer_load_b128(rws, qgo, 0, AUX_SC1);
        qb = __builtin_amdgcn_raw_buffer_load_b128(rws, qgo + 2048, 0, AUX_SC1);
        if (__builtin_amdgcn_ballot_w64((emb_bad(qa, t8) | emb_bad(qb, t8)) != 0u) == 0ull) break;
      }
    }
    *(LAS v2u*)(L + L_Q + (w * 128 + lane * 2) * 4) = v2u{pk_bf16(unemb(qa.x) + unemb(qb.x), unemb(qa.y) + unemb(qb.y)), pk_bf16(unemb(qa.z) + unemb(qb.z), unemb(qa.w) + unemb(qb.w))};
    LGKM0();
    (void)lds_inc(L + L_FLAG, lane);
    STAMP(9);
  }
  SB();
#pragma unroll
  for (int j = QPRE + 1; j < NBUF; ++j) issue(j);
  for (unsigned spin = 0; spin < (SPIN_CAP << 4); ++spin) {
    if (*(volatile LAS unsigned*)(L + L_FLAG) >= 2u) break;
    __builtin_amdgcn_s_sleep(1);
  }
  CB();
  STAMP(2);
  v4u qf[4];
#pragma unroll
  for (int i = 0; i < 4; ++i) qf[i] = *(LAS v4u*)(L + L_Q + (((c & 3) * 64 + 16 * i + 4 * g) * 4));

  constexpr float C2 = 0.08838834764831845f * 1.4426950408889634f;
  f32x4 acc[8];
#pragma unroll
  for (int e = 0; e < 8; ++e) acc[e] = f32x4{0.f, 0.f, 0.f, 0.f};
  float m = -INFINITY, l = 0.f;
#pragma unroll
  for (int j = 0; j < 8; ++j) {
    const int bf = j % NBUF;
    f32x4 sc = {0.f, 0.f, 0.f, 0.f};
#pragma unroll
    for (int i = 0; i < 4; ++i) sc = mfma32(kb[bf][i], qf[i], sc);
    const float t0 = sc[0] * C2, t1 = sc[1] * C2, t2 = sc[2] * C2, t3 = sc[3] * C2;
    const float cm = xrow_max(fmaxf(fmaxf(t0, t1), fmaxf(t2, t3)));
    const float mn = fmaxf(m, cm);
    const float alpha = __builtin_amdgcn_exp2f(m - mn);
    const float p0 = __builtin_amdgcn_exp2f(t0 - mn), p1 = __builtin_amdgcn_exp2f(t1 - mn), p2 = __builtin_amdgcn_exp2f(t2 - mn), p3 = __builtin_amdgcn_exp2f(t3 - mn);
    l = l * alpha + ((p0 + p1) + (p2 + p3));
    m = mn;
    const v2u pa = {pk_bf16(p0, p1), pk_bf16(p2, p3)};
    if (j > 0) {
      const float a0 = dpp<0x00>(alpha), a1 = dpp<0x55>(alpha), a2 = dpp<0xAA>(alpha), a3 = dpp<0xFF>(alpha);
#pragma unroll
      for (int e = 0; e < 8; ++e) { acc[e][0] *= a0; acc[e][1] *= a1; acc[e][2] *= a2; acc[e][3] *= a3; }
    }
#pragma unroll
    for (int d = 0; d < 4; ++d) {
      const v2u lo = {__builtin_amdgcn_perm(vb[bf][1][d], vb[bf][0][d], 0x05040100u), __builtin_amdgcn_perm(vb[bf][3][d], vb[bf][2][d], 0x05040100u)};
      const v2u hi = {__builtin_amdgcn_perm(vb[bf][1][d], vb[bf][0][d], 0x07060302u), __builtin_amdgcn_perm(vb[bf][3][d], vb[bf][2][d], 0x07060302u)};
      acc[2 * d] = mfma16(pa, lo, acc[2 * d]);
      acc[2 * d + 1] = mfma16(pa, hi, acc[2 * d + 1]);
    }
    if (j == QPRE) asm volatile("" :: "v"(qa), "v"(qb));
    SB();
    if (j + NBUF < 8) issue(j + NBUF);
    if (j + NBUF == 7) { if (lane == 0) (void)__hip_atomic_fetch_add((LAS unsigned*)(L + L_FLAG + 8), 1u, __ATOMIC_RELAXED, __HIP_MEMORY_SCOPE_WORKGROUP); SB(); }
  }
  STAMP(3);
  l = xrow_sum(l);
  if (g == 0) {
    LAS float* mo = (LAS float*)(L + L_MRG) + w * 512 + 8 * c;
#pragma unroll
    for (int h = 0; h < 4; ++h) {
      *(LAS f32x4*)(mo + h * 128) = f32x4{acc[0][h], acc[1][h], acc[2][h], acc[3][h]};
      *(LAS f32x4*)(mo + h * 128 + 4) = f32x4{acc[4][h], acc[5][h], acc[6][h], acc[7][h]};
    }
    if (c < 4) *(LAS f32x2*)(L + L_ML + (c * 8 + w) * 8) = f32x2{m, l};
  }
  LGKM0();
  (void)lds_inc(L + L_FLAG + 12, lane);
  auto lds_wait = [&](int off, unsigned n) {
    for (unsigned spin = 0; spin < (SPIN_CAP << 4); ++spin) {
      if (*(volatile LAS unsigned*)(L + off) >= n) break;
      __builtin_amdgcn_s_sleep(WSLP);
    }
    CB();
  };
  const f32x4 zero = {0.f, 0.f, 0.f, 0.f};
  if (w < 2) {
    for (unsigned spin = 0; spin < (SPIN_CAP << 4); ++spin) {
      if (*(volatile LAS unsigned*)(L + L_FLAG + 8) >= DTH) break;
      __builtin_amdgcn_s_sleep(1);
    }
    CB();
    v2u r0[8], r1[8], r2[8];
    v4u bo[2][4], bd[4];
    const unsigned colb = (unsigned)(cq * 128 + w * 64 + 4 * c) * 2u;
    const unsigned wdb = (unsigned)(pidx * 28 + 8 * g) * 1024u + colb;
#pragma unroll
    for (int i = 0; i < 8; ++i) r2[i] = __builtin_amdgcn_raw_buffer_load_b64(rwd, (8 * g + i < 28) ? wdb + i * 1024 : OOB, 0, W_AUX);
    const unsigned wob = (unsigned)(kvh * 512 + rb * 64 + 8 * g) * 1024u + colb;
#pragma unroll
    for (int i = 0; i < 8; ++i) r0[i] = __builtin_amdgcn_raw_buffer_load_b64(rwo, wob + i * 1024, 0, W_AUX);
#pragma unroll
    for (int i = 0; i < 8; ++i) r1[i] = __builtin_amdgcn_raw_buffer_load_b64(rwo, wob + 32768 + i * 1024, 0, W_AUX);
    SB();
    unsigned fgo = WS_FG + (unsigned)((((g * 16 + cq * 4 + w * 2 + (c >> 3)) * 64 + pidx) * 32 + (c & 7) * 4) * 4);
    lds_wait(L_FLAG2, 1u);
    const v4u hf = *(LAS v4u*)(L + L_H + ((c >> 2) * 32 + 8 * g) * 2);
    tr8x4(r2, bd);
    f32x4 db[4];
#pragma unroll
    for (int e = 0; e < 4; ++e) db[e] = mfma32(hf, bd[e], zero);
    tr8x4(r0, bo[0]); tr8x4(r1, bo[1]);
#pragma unroll
    for (int e = 0; e < 4; ++e) asm volatile("" : "+v"(bo[0][e]), "+v"(bo[1][e]), "+v"(db[e]));
    asm volatile("" : "+v"(fgo));
    STAMP(10);
    lds_wait(L_FLAG2 + 4, 4u);
    STAMP(5);
    const v4u of0 = *(LAS v4u*)(L + L_O + ((c >> 2) * 64 + 8 * g) * 2), of1 = *(LAS v4u*)(L + L_O + ((c >> 2) * 64 + 32 + 8 * g) * 2);
#pragma unroll
    for (int e = 0; e < 4; ++e) db[e] = mfma32(of0, bo[0][e], db[e]);
#pragma unroll
    for (int e = 0; e < 4; ++e) db[e] = mfma32(of1, bo[1][e], db[e]);
    __builtin_amdgcn_raw_buffer_store_b128(v4u{emb(db[0][0], t8), emb(db[1][0], t8), emb(db[2][0], t8), emb(db[3][0], t8)}, rws, fgo, 0, AUX_SC1);
    STAMP(6);
  } else if (w == 2) {
    const unsigned go = c < 7 ? WS_GU + (unsigned)((g * 1792 + pidx * 28 + c * 4) * 4) : OOB;
    v4u xv[4];
#pragma unroll
    for (int b = 0; b < 4; ++b) xv[b] = __builtin_amdgcn_raw_buffer_load_b128(rx, lane * 16 + b * 1024, 0, 0);
    const v4u gv = __builtin_amdgcn_raw_buffer_load_b128(rgw, lane * 16, 0, 0);
    v4u gq[4];
    for (unsigned spin = 0; spin < SPIN_CAP; ++spin) {
      CB();
#pragma unroll
      for (int i = 0; i < 4; ++i) gq[i] = __builtin_amdgcn_raw_buffer_load_b128(rws, go + i * 28672, 0, AUX_SC1);
      SB();
      if (__builtin_amdgcn_ballot_w64(c >= 7 || (emb_bad(gq[0], t8) | emb_bad(gq[1], t8) | emb_bad(gq[2], t8) | emb_bad(gq[3], t8)) == 0u) == ~0ull) break;
      __builtin_amdgcn_s_sleep(2);
    }
    float a[4];
#pragma unroll
    for (int b = 0; b < 4; ++b) { a[b] = dot2(xv[b].x, gv.x, 0.f); a[b] = dot2(xv[b].y, gv.y, a[b]); a[b] = dot2(xv[b].z, gv.z, a[b]); a[b] = dot2(xv[b].w, gv.w, a[b]); }
    a[0] = swap32_add(a[0], a[2]); a[1] = swap32_add(a[1], a[3]);
    a[0] = swap16_add(a[0], a[1]);
    a[0] += dpp<ROW_ROR + 8>(a[0]); a[0] += dpp<ROW_ROR + 4>(a[0]); a[0] += dpp<ROW_ROR + 2>(a[0]); a[0] += dpp<ROW_ROR + 1>(a[0]);
    const float gt = 1.f / (1.f + __expf(-a[0]));
    if (c == 0) *(LAS float*)(L + L_GATE + g * 4) = gt;
    float hh_[4];
#pragma unroll
    for (int i = 0; i < 4; ++i) hh_[i] = (1.f - gt) * (unemb(gq[2][i]) + unemb(gq[3][i])) / (1.f + __expf(-(unemb(gq[0][i]) + unemb(gq[1][i]))));
    if (c < 8) *(LAS v2u*)(L + L_H + (g * 16 + c * 2) * 4) = c < 7 ? v2u{pk_bf16(hh_[0], hh_[1]), pk_bf16(hh_[2], hh_[3])} : v2u{0u, 0u};
    LGKM0();
    if (lane == 0) *(volatile LAS unsigned*)(L + L_FLAG2) = 1u;
  } else if (w >= 4) {
    const int v = w - 4;
    const int sp = lane >> 3;
    unsigned pgo = WS_PG + (unsigned)(((kvh * 32 + r) * 512 + v * 128 + lane * 2) * 4);
    unsigned po = WS_PG + (unsigned)(((kvh * 32 + v * 8 + sp) * 512 + rb * 64 + (lane & 7) * 4) * 4);
    asm volatile("" : "+v"(pgo), "+v"(po));
    lds_wait(L_FLAG + 12, 8u);
    {
      f32x4 ml[4]; f32x2 ov[8];
#pragma unroll
      for (int i = 0; i < 4; ++i) ml[i] = *(LAS f32x4*)(L + L_ML + v * 64 + i * 16);
#pragma unroll
      for (int i = 0; i < 8; ++i) ov[i] = *(LAS f32x2*)(L + L_MRG + (i * 512 + v * 128 + lane * 2) * 4);
      SB();
      const float M = fmaxf(fmaxf(fmaxf(ml[0][0], ml[0][2]), fmaxf(ml[1][0], ml[1][2])), fmaxf(fmaxf(ml[2][0], ml[2][2]), fmaxf(ml[3][0], ml[3][2])));
      float Ls = 0.f; f32x2 o = {0.f, 0.f};
#pragma unroll
      for (int i = 0; i < 4; ++i) {
        const float w0 = __builtin_amdgcn_exp2f(ml[i][0] - M), w1 = __builtin_amdgcn_exp2f(ml[i][2] - M);
        Ls += w0 * ml[i][1] + w1 * ml[i][3]; o += w0 * ov[2 * i] + w1 * ov[2 * i + 1];
      }
      o *= __builtin_amdgcn_rcpf(Ls);
      const unsigned lseu = __float_as_uint(M + __builtin_amdgcn_logf(Ls));
      const unsigned nb2 = lseu >> ((lane & 3) * 8);
      const unsigned t4 = t8 & 0x0fu;
      __builtin_amdgcn_raw_buffer_store_b64(v2u{emb(o.x, t4 | ((nb2 & 15u) << 4)), emb(o.y, t4 | (nb2 & 0xf0u))}, rws, (EXP_HALF && (lane & 16)) ? OOB : pgo, 0, AUX_SC1);
      SB();
    }
    STAMP(4);
    {
      v4u pl0 = {0, 0, 0, 0}, pl1 = {0, 0, 0, 0};
      bool pok = false;
      auto chk1 = [&](const v4u& x, const v4u& y, bool live) -> bool {
        const bool mine = live && ((((x.x ^ t8) | (x.y ^ t8) | (x.z ^ t8) | (x.w ^ t8)) | ((y.x ^ t8) | (y.y ^ t8) | (y.z ^ t8) | (y.w ^ t8))) & 0x0fu) == 0u;
        const unsigned long long bm = __builtin_amdgcn_ballot_w64(mine);
        const bool take = ((unsigned)(bm >> (lane & 56)) & 0xffu) == 0xffu;
#pragma unroll
        for (int i = 0; i < 4; ++i) { pl0[i] = take ? x[i] : pl0[i]; pl1[i] = take ? y[i] : pl1[i]; }
        pok = pok || take;
        return __builtin_amdgcn_ballot_w64(!pok) == 0ull;
      };
      lds_wait(L_FLAG2, 1u);
      const float gate = *(LAS float*)(L + L_GATE + v * 4);
      for (unsigned spin = 0; spin < SPIN_CAP; ++spin) {
        CB();
        const bool lv = !pok;
        const unsigned o_ = lv ? po : OOB;
        const v4u a0 = __builtin_amdgcn_raw_buffer_load_b128(rws, o_, 0, AUX_SC1);
        const v4u a1 = EXP_HALF ? a0 : __builtin_amdgcn_raw_buffer_load_b128(rws, o_ + 128, 0, AUX_SC1);
        SB();
        if (chk1(a0, a1, lv)) break;
      }
      const unsigned half16 = ((pl0.x >> 4) & 15u) | (pl0.y & 0xf0u) | ((pl0.z & 0xf0u) << 4) | ((pl0.w & 0xf0u) << 8);
      const unsigned other = __builtin_amdgcn_mov_dpp(half16, 0xB1, 0xf, 0xf, true);
      const float lse = __uint_as_float((lane & 1) ? (other | (half16 << 16)) : (half16 | (other << 16)));
      float M = fmaxf(lse, dpp<ROW_ROR + 8>(lse)); M = xrow_max(M);
      const float wt_ = __builtin_amdgcn_exp2f(lse - M);
      float W = wt_; W += dpp<ROW_ROR + 8>(W); W = xrow_sum(W);
      float u[4];
#pragma unroll
      for (int i = 0; i < 4; ++i) u[i] = swap32_add(wt_ * unemb(pl0[i]), wt_ * unemb(pl1[i]));
      u[0] = swap16_add(u[0], u[2]); u[1] = swap16_add(u[1], u[3]);
      u[0] += dpp<ROW_ROR + 8>(u[0]); u[1] += dpp<ROW_ROR + 8>(u[1]);
      const float inv = gate * __builtin_amdgcn_rcpf(W);
      if ((lane & 8) == 0) *(LAS unsigned*)(L + L_O + (v * 64 + (lane >> 5) * 32 + (lane & 7) * 4 + ((lane >> 4) & 1) * 2) * 2) = pk_bf16(u[0] * inv, u[1] * inv);
      LGKM0();
      (void)lds_inc(L + L_FLAG2 + 4, lane);
      STAMP(1);
    }
    if ((r & 3) == 0) {
      const int R = (r >> 2) * 8 + kvh;
      const unsigned fo = WS_FG + (unsigned)(R * 8192 + v * 2048 + lane * 16);
      v4u pl0 = {0, 0, 0, 0}, pl1 = {0, 0, 0, 0};
      bool ok0 = false, ok1 = false;
      auto chk2 = [&](const v4u& x, const v4u& y, bool l0, bool l1) -> bool {
        const bool t0_ = l0 && emb_ok(x, t8), t1_ = l1 && emb_ok(y, t8);
#pragma unroll
        for (int i = 0; i < 4; ++i) { pl0[i] = t0_ ? x[i] : pl0[i]; pl1[i] = t1_ ? y[i] : pl1[i]; }
        ok0 = ok0 || t0_; ok1 = ok1 || t1_;
        return __builtin_amdgcn_ballot_w64(!(ok0 && ok1)) == 0ull;
      };
      for (unsigned spin = 0; spin < SPIN_CAP; ++spin) {
        CB();
        const bool l0 = !ok0, l1 = !ok1;
        const v4u c0 = __builtin_amdgcn_raw_buffer_load_b128(rws, l0 ? fo : OOB, 0, AUX_SC1);
        const v4u c1 = __builtin_amdgcn_raw_buffer_load_b128(rws, l1 ? fo + 1024 : OOB, 0, AUX_SC1);
        SB();
        if (chk2(c0, c1, l0, l1)) break;
      }
      float v0 = unemb(pl0.x) + unemb(pl1.x), v1 = unemb(pl0.y) + unemb(pl1.y), v2 = unemb(pl0.z) + unemb(pl1.z), v3 = unemb(pl0.w) + unemb(pl1.w);
      v0 = swap32_add(v0, v2); v1 = swap32_add(v1, v3);
      v0 = swap16_add(v0, v0); v1 = swap16_add(v1, v1);
      v0 += dpp<ROW_ROR + 8>(v0); v1 += dpp<ROW_ROR + 8>(v1);
      if ((lane & 24) == 0) *(LAS f32x2*)(L + L_RED + (v * 32 + (lane & 7) * 4 + (lane >> 5) * 2) * 4) = f32x2{v0, v1};
      LGKM0();
      const unsigned old = lds_inc(L + L_FLAG2 + 8, lane);
      CB();
      if (old == 3 && lane < 16) {
        const LAS f32x2* fr = (const LAS f32x2*)(L + L_RED) + lane;
        const f32x2 s = (fr[0] + fr[16]) + (fr[32] + fr[48]);
        __builtin_amdgcn_raw_buffer_store_b32(pk_bf16(s.x, s.y), mk(pout, 4096), (R * 16 + lane) * 4, 0, 0);
        __builtin_amdgcn_raw_buffer_store_b32(tag, rws, (blk == 0 && lane == 0) ? 0u : OOB, 0, AUX_SC1);
      }
    }
  }
  STAMP(7);
  }
}
}

extern "C" void kernel_launch(void* const* d_in, const int* in_sizes, int n_in, void* d_out, int out_size, void* d_ws,
                              size_t ws_size, hipStream_t stream) {
  fk::fused_kernel<<<256, 512, 0, stream>>>(d_in[0], d_in[1], d_in[2], d_in[3], d_in[4], d_in[6], d_in[7], d_in[8], d_in[9], d_out, d_ws);
}
```

```cpp
#define SLP1 0
#define SLP2 0
#define WSLP 1
#define W_AUX 0
#include <hip/hip_runtime.h>
#include <cstdint>

#define K_AUX 2
#define V_AUX 2
#define DEFER 1
#define PIPE 1
#define GAP1 0
#define GAP2 0
#define DTH 8u
#define EXP_HALF 0
#define KJM 0
#define VJM 0
#define RND 1
#define NBUF 5
#define STAMPS 0
#define QPRE 4
#define PREK 4
#define PREV 4
#define EXP_NOTILE 0
#define EXP_NOTW 0
#define EXP_NOX 0
#define EXP_NOQ 0
#define X_AUX 0
#define SPEC 2

typedef unsigned v4u __attribute__((ext_vector_type(4)));
typedef unsigned v2u __attribute__((ext_vector_type(2)));
typedef float f32x4 __attribute__((ext_vector_type(4)));
typedef float f32x2 __attribute__((ext_vector_type(2)));
typedef short s16x4 __attribute__((ext_vector_type(4)));
typedef __bf16 bf16x8 __attribute__((ext_vector_type(8)));
typedef __bf16 bf16x2 __attribute__((ext_vector_type(2)));
typedef __amdgpu_buffer_rsrc_t rsrc_t;
#define LAS __attribute__((address_space(3)))

namespace fk {
constexpr int AUX_NT = 2, AUX_SC1 = 16;
constexpr unsigned OOB = 0x40000000u;
constexpr unsigned WS_QG = 4096;
constexpr unsigned WS_GU = WS_QG + 131072;
constexpr unsigned WS_PG = WS_GU + 114688;
constexpr unsigned WS_FG = WS_PG + 524288;
constexpr unsigned WS_END = WS_FG + 524288;
constexpr unsigned WS_STAMP = 8u << 20;
constexpr unsigned SPIN_CAP = 1u << 17;

constexpr int L_RED = 0;
constexpr int L_Q = 8192;
constexpr int L_H = L_Q + 1024;
constexpr int L_GATE = L_H + 256;
constexpr int L_FLAG = L_GATE + 16;
constexpr int L_FLAG2 = L_FLAG + 16;
constexpr int L_O = L_FLAG2 + 16;
constexpr int L_ML = L_O + 512;
constexpr int L_MRG = L_ML + 256;
constexpr int L_END = L_MRG + 16384;

__device__ __forceinline__ rsrc_t mk(const void* p, unsigned bytes) {
  return __builtin_amdgcn_make_buffer_rsrc((void*)p, 0, (int)bytes, 0x00020000);
}
__device__ __forceinline__ float dot2(unsigned a, unsigned b, float c) {
  return __builtin_amdgcn_fdot2_f32_bf16(__builtin_bit_cast(bf16x2, a), __builtin_bit_cast(bf16x2, b), c, false);
}
__device__ __forceinline__ unsigned pk_bf16(float lo, float hi) {
  f32x2 v = {lo, hi};
  return __builtin_bit_cast(unsigned, __builtin_convertvector(v, bf16x2));
}
template <int CTRL> __device__ __forceinline__ float dpp(float x) {
  return __builtin_bit_cast(float, __builtin_amdgcn_mov_dpp(__builtin_bit_cast(int, x), CTRL, 0xf, 0xf, true));
}
__device__ __forceinline__ float swap32_add(float a, float b) {
  auto s = __builtin_amdgcn_permlane32_swap(__float_as_uint(a), __float_as_uint(b), false, false);
  return __uint_as_float(s[0]) + __uint_as_float(s[1]);
}
__device__ __forceinline__ float swap16_add(float a, float b) {
  auto s = __builtin_amdgcn_permlane16_swap(__float_as_uint(a), __float_as_uint(b), false, false);
  return __uint_as_float(s[0]) + __uint_as_float(s[1]);
}
__device__ __forceinline__ float xrow_max(float x) {
  auto s = __builtin_amdgcn_permlane16_swap(__float_as_uint(x), __float_as_uint(x), false, false);
  x = fmaxf(__uint_as_float(s[0]), __uint_as_float(s[1]));
  auto t = __builtin_amdgcn_permlane32_swap(__float_as_uint(x), __float_as_uint(x), false, false);
  return fmaxf(__uint_as_float(t[0]), __uint_as_float(t[1]));
}
__device__ __forceinline__ float xrow_sum(float x) {
  x = swap16_add(x, x);
  return swap32_add(x, x);
}
constexpr int ROW_SHL = 0x100, ROW_SHR = 0x110, ROW_ROR = 0x120;
__device__ __forceinline__ f32x4 treduce8(float (&v)[32], int lane) {
#pragma unroll
  for (int i = 0; i < 16; ++i) v[i] = swap32_add(v[i], v[i + 16]);
#pragma unroll
  for (int i = 0; i < 8; ++i) v[i] = swap16_add(v[i], v[i + 8]);
#pragma unroll
  for (int i = 0; i < 4; ++i) { float t1 = v[i] + dpp<ROW_ROR + 8>(v[i]), t2 = v[i + 4] + dpp<ROW_ROR + 8>(v[i + 4]); v[i] = (lane & 8) ? t2 : t1; }
  return f32x4{v[0], v[1], v[2], v[3]};
}
template <bool FIRST>
__device__ __forceinline__ void rowpair(float (&v)[32], const v4u& w0, const v4u& w1, unsigned x0, unsigned x1, unsigned x2, unsigned x3) {
  const unsigned xp[4] = {x0, x1, x2, x3};
#pragma unroll
  for (int d = 0; d < 4; ++d) {
    const unsigned lo = __builtin_amdgcn_perm(w1[d], w0[d], 0x05040100u), hi = __builtin_amdgcn_perm(w1[d], w0[d], 0x07060302u);
#pragma unroll
    for (int b = 0; b < 4; ++b) {
      v[b * 8 + 2 * d] = dot2(lo, xp[b], FIRST ? 0.f : v[b * 8 + 2 * d]);
      v[b * 8 + 2 * d + 1] = dot2(hi, xp[b], FIRST ? 0.f : v[b * 8 + 2 * d + 1]);
    }
  }
}
__device__ __forceinline__ f32x4 mfma32(v4u a, v4u b, f32x4 c) {
  return __builtin_amdgcn_mfma_f32_16x16x32_bf16(__builtin_bit_cast(bf16x8, a), __builtin_bit_cast(bf16x8, b), c, 0, 0, 0);
}
__device__ __forceinline__ f32x4 mfma16(v2u a, v2u b, f32x4 c) {
  return __builtin_amdgcn_mfma_f32_16x16x16bf16_1k(__builtin_bit_cast(s16x4, a), __builtin_bit_cast(s16x4, b), c, 0, 0, 0);
}
__device__ __forceinline__ float sel4(const f32x4& d, int g) { return g == 0 ? d[0] : g == 1 ? d[1] : g == 2 ? d[2] : d[3]; }
__device__ __forceinline__ unsigned lds_inc(LAS unsigned char* p, int lane) {
  unsigned old = 0;
  if (lane == 0) old = __hip_atomic_fetch_add((LAS unsigned*)p, 1u, __ATOMIC_RELAXED, __HIP_MEMORY_SCOPE_WORKGROUP);
  return __builtin_amdgcn_readfirstlane(old);
}
__device__ __forceinline__ unsigned emb(float v, unsigned t8) { return ((__float_as_uint(v) + 0x80u) & 0xffffff00u) | t8; }
__device__ __forceinline__ bool emb_ok(const v4u& a, unsigned t8) { return (((a.x ^ t8) | (a.y ^ t8) | (a.z ^ t8) | (a.w ^ t8)) & 0xffu) == 0u; }
__device__ __forceinline__ unsigned emb_bad(const v4u& a, unsigned t8) { return ((a.x ^ t8) | (a.y ^ t8) | (a.z ^ t8) | (a.w ^ t8)) & 0xffu; }
__device__ __forceinline__ float unemb(unsigned u) { return __uint_as_float(u & 0xffffff00u); }
__device__ __forceinline__ void tr8x4(const v2u (&r)[8], v4u (&f)[4]) {
#pragma unroll
  for (int i = 0; i < 4; ++i) {
    f[0][i] = __builtin_amdgcn_perm(r[2 * i + 1].x, r[2 * i].x, 0x05040100u); f[1][i] = __builtin_amdgcn_perm(r[2 * i + 1].x, r[2 * i].x, 0x07060302u);
    f[2][i] = __builtin_amdgcn_perm(r[2 * i + 1].y, r[2 * i].y, 0x05040100u); f[3][i] = __builtin_amdgcn_perm(r[2 * i + 1].y, r[2 * i].y, 0x07060302u);
  }
}
#define SB() __builtin_amdgcn_sched_barrier(0)
#define CB() asm volatile("" ::: "memory")
#define LGKM0() asm volatile("s_waitcnt lgkmcnt(0)" ::: "memory")
#define BAR() do { LGKM0(); __builtin_amdgcn_s_barrier(); CB(); } while (0)
#define STAMP(i) do { } while (0)

__global__ void __launch_bounds__(512, 2)
fused_kernel(const void* __restrict__ px, const void* __restrict__ pk, const void* __restrict__ pv, const void* __restrict__ pwq,
             const void* __restrict__ pwo, const void* __restrict__ pwg, const void* __restrict__ pwu, const void* __restrict__ pwd,
             const void* __restrict__ pgw, void* __restrict__ pout, void* pws) {
  __shared__ __attribute__((aligned(16))) unsigned char smem[L_END];
  LAS unsigned char* L = (LAS unsigned char*)smem;
  STAMP(0);
  asm volatile("" :: "s"(px), "s"(pk), "s"(pv), "s"(pwq), "s"(pwo), "s"(pwg), "s"(pwu), "s"(pwd), "s"(pgw), "s"(pout), "s"(pws));
  const int tid = threadIdx.x, lane = tid & 63, w = __builtin_amdgcn_readfirstlane(tid >> 6);
  const int g = lane >> 4, c = lane & 15;
  const int blk = blockIdx.x, kvh = blk & 7, r = blk >> 3;
  const int b_att = r >> 3, s_att = r & 7;
  const int rb = r >> 2, cq = r & 3;
  const int pidx = kvh * 8 + rb;

  const size_t kvoff = ((size_t)(b_att * 8 + kvh) * 8192 + s_att * 1024) * 256;
  const rsrc_t rk = mk((const char*)pk + kvoff, 262144), rv = mk((const char*)pv + kvoff, 262144);
  const rsrc_t rws = mk(pws, WS_END);
  const rsrc_t rx = mk(px, 4096), rgw = mk(pgw, 1024);
  const rsrc_t rwo = mk(pwo, 4096 * 512 * 2), rwd = mk(pwd, 1792 * 512 * 2);

  __builtin_amdgcn_s_setprio(3);
  const bool isq = r < 16;
  const int gu = kvh * 14 + (r - 16);
  const int um = gu >= 56 ? 1 : 0;
  const int ulc = isq ? kvh * 8 + (r >> 1) : ((gu - 56 * um) >> 1);
  const int uh = isq ? (r & 1) : (gu & 1);
  v4u wt[4]; v2u xq[4];
  {
    const int row = uh * 256 + w * 32 + 4 * (lane >> 3);
    const unsigned rstr = isq ? 8192u : 3584u;
    const rsrc_t ru = mk(isq ? pwq : (um ? pwu : pwg), isq ? 512 * 4096 * 2 : 512 * 1792 * 2);
    const unsigned uo = r < 30 ? (unsigned)row * rstr + (unsigned)(ulc * 128 + (lane & 7) * 16) : OOB;
#pragma unroll
    for (int i = 0; i < 4; ++i) wt[i] = __builtin_amdgcn_raw_buffer_load_b128(ru, uo + i * rstr, 0, W_AUX);
#pragma unroll
    for (int b = 0; b < 4; ++b) xq[b] = __builtin_amdgcn_raw_buffer_load_b64(rx, row * 2 + b * 1024, 0, X_AUX);
  }
  SB();
  if (tid == 0) { *(volatile LAS v4u*)(L + L_FLAG) = v4u{0u, 0u, 0u, 0u}; *(volatile LAS v4u*)(L + L_FLAG2) = v4u{0u, 0u, 0u, 0u}; }
  BAR();
  SB();
  const unsigned ko = c * 256 + g * 16, vo = g * 1024 + c * 16;
  v4u kb[NBUF][4], vb[NBUF][4];
  auto issueK = [&](int j, int i0, int i1) {
    const unsigned so = (unsigned)(j * 8 + w) * 4096u;
#pragma unroll
    for (int i = i0; i < i1; ++i) kb[j % NBUF][i] = ((KJM >> j) & 1) ? __builtin_amdgcn_raw_buffer_load_b128(rk, ko + i * 64, so, 0) : __builtin_amdgcn_raw_buffer_load_b128(rk, ko + i * 64, so, 2);
    SB();
  };
  auto issueV = [&](int j, int i0, int i1) {
    const unsigned so = (unsigned)(j * 8 + w) * 4096u;
#pragma unroll
    for (int i = i0; i < i1; ++i) vb[j % NBUF][i] = ((VJM >> j) & 1) ? __builtin_amdgcn_raw_buffer_load_b128(rv, vo + i * 256, so, 0) : __builtin_amdgcn_raw_buffer_load_b128(rv, vo + i * 256, so, 2);
    SB();
  };
  auto issue = [&](int j) { issueK(j, 0, 4); issueV(j, 0, 4); STAMP(11 + j); };
  const unsigned E = *(const __attribute__((address_space(4))) unsigned*)pws;
  SB();
  issueV(0, 0, PREV);
  issueK(0, 0, PREK);
  const unsigned tag = E * 0x9E3779B5u + 0x7F4A7C15u;
  const unsigned t8 = tag & 0xffu;
  {
    LAS f32x4* red = (LAS f32x4*)(L + L_RED);
    float v[32];
    rowpair<true>(v, wt[0], wt[1], xq[0].x, xq[1].x, xq[2].x, xq[3].x);
    rowpair<false>(v, wt[2], wt[3], xq[0].y, xq[1].y, xq[2].y, xq[3].y);
    red[w * 64 + lane] = treduce8(v, lane);
    LGKM0();
    const unsigned old = lds_inc(L + L_FLAG + 4, lane);
    CB();
    const f32x4 s = ((red[lane] + red[64 + lane]) + (red[128 + lane] + red[192 + lane])) + ((red[256 + lane] + red[320 + lane]) + (red[384 + lane] + red[448 + lane]));
    const int c64 = (lane & 7) * 8 + ((lane >> 3) & 1) * 4;
    const unsigned po = isq ? WS_QG + (unsigned)((((kvh * 4 + g) * 2 + uh) * 512 + (r >> 1) * 64 + c64) * 4)
                            : WS_GU + (unsigned)((((um * 2 + uh) * 4 + g) * 1792 + ulc * 64 + c64) * 4);
    __builtin_amdgcn_raw_buffer_store_b128(v4u{emb(s[0], t8), emb(s[1], t8), emb(s[2], t8), emb(s[3], t8)}, rws, (old == 7 && r < 30) ? po : OOB, 0, AUX_SC1);
    SB();
    __builtin_amdgcn_s_setprio(0);
    SB();
  }
  issueV(0, PREV, 4);
  issueK(0, PREK, 4);
  STAMP(11);
#pragma unroll
  for (int j = 1; j < QPRE; ++j) issue(j);
  const unsigned qgo = w < 2 ? WS_QG + (unsigned)((kvh * 4 + b_att) * 4096 + w * 1024 + lane * 16) : OOB;
  CB();
  v4u qa = __builtin_amdgcn_raw_buffer_load_b128(rws, qgo, 0, AUX_SC1);
  v4u qb = __builtin_amdgcn_raw_buffer_load_b128(rws, qgo + 2048, 0, AUX_SC1);
  SB();
  issue(QPRE);
  if (w < 2) {
    if (__builtin_amdgcn_ballot_w64((emb_bad(qa, t8) | emb_bad(qb, t8)) != 0u) != 0ull) {
      for (unsigned spin = 0; spin < SPIN_CAP; ++spin) {
        CB();
        qa = __builtin_amdgcn_raw_buffer_load_b128(rws, qgo, 0, AUX_SC1);
        qb = __builtin_amdgcn_raw_buffer_load_b128(rws, qgo + 2048, 0, AUX_SC1);
        if (__builtin_amdgcn_ballot_w64((emb_bad(qa, t8) | emb_bad(qb, t8)) != 0u) == 0ull) break;
      }
    }
    *(LAS v2u*)(L + L_Q + (w * 128 + lane * 2) * 4) = v2u{pk_bf16(unemb(qa.x) + unemb(qb.x), unemb(qa.y) + unemb(qb.y)), pk_bf16(unemb(qa.z) + unemb(qb.z), unemb(qa.w) + unemb(qb.w))};
    LGKM0();
    (void)lds_inc(L + L_FLAG, lane);
    STAMP(9);
  }
  SB();
#pragma unroll
  for (int j = QPRE + 1; j < NBUF; ++j) issue(j);
  for (unsigned spin = 0; spin < (SPIN_CAP << 4); ++spin) {
    if (*(volatile LAS unsigned*)(L + L_FLAG) >= 2u) break;
    __builtin_amdgcn_s_sleep(1);
  }
  CB();
  STAMP(2);
  v4u qf[4];
#pragma unroll
  for (int i = 0; i < 4; ++i) qf[i] = *(LAS v4u*)(L + L_Q + (((c & 3) * 64 + 16 * i + 4 * g) * 4));

  constexpr float C2 = 0.08838834764831845f * 1.4426950408889634f;
  f32x4 acc[8];
#pragma unroll
  for (int e = 0; e < 8; ++e) acc[e] = f32x4{0.f, 0.f, 0.f, 0.f};
  float m = -INFINITY, l = 0.f;
#pragma unroll
  for (int j = 0; j < 8; ++j) {
    const int bf = j % NBUF;
    f32x4 sc = {0.f, 0.f, 0.f, 0.f};
#pragma unroll
    for (int i = 0; i < 4; ++i) sc = mfma32(kb[bf][i], qf[i], sc);
    const unsigned s01 = pk_bf16(sc[0], sc[1]), s23 = pk_bf16(sc[2], sc[3]);
    constexpr float SCL = 0.08838834764831845f, L2E = 1.4426950408889634f;
    const unsigned u01 = pk_bf16(__uint_as_float(s01 << 16) * SCL, __uint_as_float(s01 & 0xffff0000u) * SCL);
    const unsigned u23 = pk_bf16(__uint_as_float(s23 << 16) * SCL, __uint_as_float(s23 & 0xffff0000u) * SCL);
    const float t0 = __uint_as_float(u01 << 16) * L2E, t1 = __uint_as_float(u01 & 0xffff0000u) * L2E, t2 = __uint_as_float(u23 << 16) * L2E, t3 = __uint_as_float(u23 & 0xffff0000u) * L2E;
    const float cm = xrow_max(fmaxf(fmaxf(t0, t1), fmaxf(t2, t3)));
    const float mn = fmaxf(m, cm);
    const float alpha = __builtin_amdgcn_exp2f(m - mn);
    const float p0 = __builtin_amdgcn_exp2f(t0 - mn), p1 = __builtin_amdgcn_exp2f(t1 - mn), p2 = __builtin_amdgcn_exp2f(t2 - mn), p3 = __builtin_amdgcn_exp2f(t3 - mn);
    l = l * alpha + ((p0 + p1) + (p2 + p3));
    m = mn;
    const v2u pa = {pk_bf16(p0, p1), pk_bf16(p2, p3)};
    if (j > 0) {
      const float a0 = dpp<0x00>(alpha), a1 = dpp<0x55>(alpha), a2 = dpp<0xAA>(alpha), a3 = dpp<0xFF>(alpha);
#pragma unroll
      for (int e = 0; e < 8; ++e) { acc[e][0] *= a0; acc[e][1] *= a1; acc[e][2] *= a2; acc[e][3] *= a3; }
    }
#pragma unroll
    for (int d = 0; d < 4; ++d) {
      const v2u lo = {__builtin_amdgcn_perm(vb[bf][1][d], vb[bf][0][d], 0x05040100u), __builtin_amdgcn_perm(vb[bf][3][d], vb[bf][2][d], 0x05040100u)};
      const v2u hi = {__builtin_amdgcn_perm(vb[bf][1][d], vb[bf][0][d], 0x07060302u), __builtin_amdgcn_perm(vb[bf][3][d], vb[bf][2][d], 0x07060302u)};
      acc[2 * d] = mfma16(pa, lo, acc[2 * d]);
      acc[2 * d + 1] = mfma16(pa, hi, acc[2 * d + 1]);
    }
    if (j == QPRE) asm volatile("" :: "v"(qa), "v"(qb));
    SB();
    if (j + NBUF < 8) issue(j + NBUF);
    if (j + NBUF == 7) { if (lane == 0) (void)__hip_atomic_fetch_add((LAS unsigned*)(L + L_FLAG + 8), 1u, __ATOMIC_RELAXED, __HIP_MEMORY_SCOPE_WORKGROUP); SB(); }
  }
  STAMP(3);
  l = xrow_sum(l);
  if (g == 0) {
    LAS float* mo = (LAS float*)(L + L_MRG) + w * 512 + 8 * c;
#pragma unroll
    for (int h = 0; h < 4; ++h) {
      *(LAS f32x4*)(mo + h * 128) = f32x4{acc[0][h], acc[1][h], acc[2][h], acc[3][h]};
      *(LAS f32x4*)(mo + h * 128 + 4) = f32x4{acc[4][h], acc[5][h], acc[6][h], acc[7][h]};
    }
    if (c < 4) *(LAS f32x2*)(L + L_ML + (c * 8 + w) * 8) = f32x2{m, l};
  }
  LGKM0();
  (void)lds_inc(L + L_FLAG + 12, lane);
  auto lds_wait = [&](int off, unsigned n) {
    for (unsigned spin = 0; spin < (SPIN_CAP << 4); ++spin) {
      if (*(volatile LAS unsigned*)(L + off) >= n) break;
      __builtin_amdgcn_s_sleep(WSLP);
    }
    CB();
  };
  const f32x4 zero = {0.f, 0.f, 0.f, 0.f};
  if (w < 2) {
    for (unsigned spin = 0; spin < (SPIN_CAP << 4); ++spin) {
      if (*(volatile LAS unsigned*)(L + L_FLAG + 8) >= DTH) break;
      __builtin_amdgcn_s_sleep(1);
    }
    CB();
    v2u r0[8], r1[8], r2[8];
    v4u bo[2][4], bd[4];
    const unsigned colb = (unsigned)(cq * 128 + w * 64 + 4 * c) * 2u;
    const unsigned wdb = (unsigned)(pidx * 28 + 8 * g) * 1024u + colb;
#pragma unroll
    for (int i = 0; i < 8; ++i) r2[i] = __builtin_amdgcn_raw_buffer_load_b64(rwd, (8 * g + i < 28) ? wdb + i * 1024 : OOB, 0, W_AUX);
    const unsigned wob = (unsigned)(kvh * 512 + rb * 64 + 8 * g) * 1024u + colb;
#pragma unroll
    for (int i = 0; i < 8; ++i) r0[i] = __builtin_amdgcn_raw_buffer_load_b64(rwo, wob + i * 1024, 0, W_AUX);
#pragma unroll
    for (int i = 0; i < 8; ++i) r1[i] = __builtin_amdgcn_raw_buffer_load_b64(rwo, wob + 32768 + i * 1024, 0, W_AUX);
    SB();
    unsigned fgo = WS_FG + (unsigned)((((g * 16 + cq * 4 + w * 2 + (c >> 3)) * 64 + pidx) * 32 + (c & 7) * 4) * 4);
    lds_wait(L_FLAG2, 1u);
    const v4u hf = *(LAS v4u*)(L + L_H + ((c >> 2) * 32 + 8 * g) * 2);
    tr8x4(r2, bd);
    f32x4 db[4];
#pragma unroll
    for (int e = 0; e < 4; ++e) db[e] = mfma32(hf, bd[e], zero);
    tr8x4(r0, bo[0]); tr8x4(r1, bo[1]);
#pragma unroll
    for (int e = 0; e < 4; ++e) asm volatile("" : "+v"(bo[0][e]), "+v"(bo[1][e]), "+v"(db[e]));
    asm volatile("" : "+v"(fgo));
    STAMP(10);
    lds_wait(L_FLAG2 + 4, 4u);
    STAMP(5);
    const v4u of0 = *(LAS v4u*)(L + L_O + ((c >> 2) * 64 + 8 * g) * 2), of1 = *(LAS v4u*)(L + L_O + ((c >> 2) * 64 + 32 + 8 * g) * 2);
#pragma unroll
    for (int e = 0; e < 4; ++e) db[e] = mfma32(of0, bo[0][e], db[e]);
#pragma unroll
    for (int e = 0; e < 4; ++e) db[e] = mfma32(of1, bo[1][e], db[e]);
    __builtin_amdgcn_raw_buffer_store_b128(v4u{emb(db[0][0], t8), emb(db[1][0], t8), emb(db[2][0], t8), emb(db[3][0], t8)}, rws, fgo, 0, AUX_SC1);
    STAMP(6);
  } else if (w == 2) {
    const unsigned go = c < 7 ? WS_GU + (unsigned)((g * 1792 + pidx * 28 + c * 4) * 4) : OOB;
    v4u xv[4];
#pragma unroll
    for (int b = 0; b < 4; ++b) xv[b] = __builtin_amdgcn_raw_buffer_load_b128(rx, lane * 16 + b * 1024, 0, 0);
    const v4u gv = __builtin_amdgcn_raw_buffer_load_b128(rgw, lane * 16, 0, 0);
    v4u gq[4];
    for (unsigned spin = 0; spin < SPIN_CAP; ++spin) {
      CB();
#pragma unroll
      for (int i = 0; i < 4; ++i) gq[i] = __builtin_amdgcn_raw_buffer_load_b128(rws, go + i * 28672, 0, AUX_SC1);
      SB();
      if (__builtin_amdgcn_ballot_w64(c >= 7 || (emb_bad(gq[0], t8) | emb_bad(gq[1], t8) | emb_bad(gq[2], t8) | emb_bad(gq[3], t8)) == 0u) == ~0ull) break;
      __builtin_amdgcn_s_sleep(2);
    }
    float a[4];
#pragma unroll
    for (int b = 0; b < 4; ++b) { a[b] = dot2(xv[b].x, gv.x, 0.f); a[b] = dot2(xv[b].y, gv.y, a[b]); a[b] = dot2(xv[b].z, gv.z, a[b]); a[b] = dot2(xv[b].w, gv.w, a[b]); }
    a[0] = swap32_add(a[0], a[2]); a[1] = swap32_add(a[1], a[3]);
    a[0] = swap16_add(a[0], a[1]);
    a[0] += dpp<ROW_ROR + 8>(a[0]); a[0] += dpp<ROW_ROR + 4>(a[0]); a[0] += dpp<ROW_ROR + 2>(a[0]); a[0] += dpp<ROW_ROR + 1>(a[0]);
    const float gt = 1.f / (1.f + __expf(-a[0]));
    if (c == 0) *(LAS float*)(L + L_GATE + g * 4) = gt;
    float hh_[4];
#pragma unroll
    for (int i = 0; i < 4; ++i) hh_[i] = (1.f - gt) * (unemb(gq[2][i]) + unemb(gq[3][i])) / (1.f + __expf(-(unemb(gq[0][i]) + unemb(gq[1][i]))));
    if (c < 8) *(LAS v2u*)(L + L_H + (g * 16 + c * 2) * 4) = c < 7 ? v2u{pk_bf16(hh_[0], hh_[1]), pk_bf16(hh_[2], hh_[3])} : v2u{0u, 0u};
    LGKM0();
    if (lane == 0) *(volatile LAS unsigned*)(L + L_FLAG2) = 1u;
  } else if (w >= 4) {
    const int v = w - 4;
    const int sp = lane >> 3;
    unsigned pgo = WS_PG + (unsigned)(((kvh * 32 + r) * 512 + v * 128 + lane * 2) * 4);
    unsigned po = WS_PG + (unsigned)(((kvh * 32 + v * 8 + sp) * 512 + rb * 64 + (lane & 7) * 4) * 4);
    asm volatile("" : "+v"(pgo), "+v"(po));
    lds_wait(L_FLAG + 12, 8u);
    {
      f32x4 ml[4]; f32x2 ov[8];
#pragma unroll
      for (int i = 0; i < 4; ++i) ml[i] = *(LAS f32x4*)(L + L_ML + v * 64 + i * 16);
#pragma unroll
      for (int i = 0; i < 8; ++i) ov[i] = *(LAS f32x2*)(L + L_MRG + (i * 512 + v * 128 + lane * 2) * 4);
      SB();
      const float M = fmaxf(fmaxf(fmaxf(ml[0][0], ml[0][2]), fmaxf(ml[1][0], ml[1][2])), fmaxf(fmaxf(ml[2][0], ml[2][2]), fmaxf(ml[3][0], ml[3][2])));
      float Ls = 0.f; f32x2 o = {0.f, 0.f};
#pragma unroll
      for (int i = 0; i < 4; ++i) {
        const float w0 = __builtin_amdgcn_exp2f(ml[i][0] - M), w1 = __builtin_amdgcn_exp2f(ml[i][2] - M);
        Ls += w0 * ml[i][1] + w1 * ml[i][3]; o += w0 * ov[2 * i] + w1 * ov[2 * i + 1];
      }
      o *= __builtin_amdgcn_rcpf(Ls);
      const unsigned lseu = __float_as_uint(M + __builtin_amdgcn_logf(Ls));
      const unsigned nb2 = lseu >> ((lane & 3) * 8);
      const unsigned t4 = t8 & 0x0fu;
      __builtin_amdgcn_raw_buffer_store_b64(v2u{emb(o.x, t4 | ((nb2 & 15u) << 4)), emb(o.y, t4 | (nb2 & 0xf0u))}, rws, (EXP_HALF && (lane & 16)) ? OOB : pgo, 0, AUX_SC1);
      SB();
    }
    STAMP(4);
    {
      v4u pl0 = {0, 0, 0, 0}, pl1 = {0, 0, 0, 0};
      bool pok = false;
      auto chk1 = [&](const v4u& x, const v4u& y, bool live) -> bool {
        const bool mine = live && ((((x.x ^ t8) | (x.y ^ t8) | (x.z ^ t8) | (x.w ^ t8)) | ((y.x ^ t8) | (y.y ^ t8) | (y.z ^ t8) | (y.w ^ t8))) & 0x0fu) == 0u;
        const unsigned long long bm = __builtin_amdgcn_ballot_w64(mine);
        const bool take = ((unsigned)(bm >> (lane & 56)) & 0xffu) == 0xffu;
#pragma unroll
        for (int i = 0; i < 4; ++i) { pl0[i] = take ? x[i] : pl0[i]; pl1[i] = take ? y[i] : pl1[i]; }
        pok = pok || take;
        return __builtin_amdgcn_ballot_w64(!pok) == 0ull;
      };
      lds_wait(L_FLAG2, 1u);
      const float gate = *(LAS float*)(L + L_GATE + v * 4);
      for (unsigned spin = 0; spin < SPIN_CAP; ++spin) {
        CB();
        const bool lv = !pok;
        const unsigned o_ = lv ? po : OOB;
        const v4u a0 = __builtin_amdgcn_raw_buffer_load_b128(rws, o_, 0, AUX_SC1);
        const v4u a1 = EXP_HALF ? a0 : __builtin_amdgcn_raw_buffer_load_b128(rws, o_ + 128, 0, AUX_SC1);
        SB();
        if (chk1(a0, a1, lv)) break;
      }
      const unsigned half16 = ((pl0.x >> 4) & 15u) | (pl0.y & 0xf0u) | ((pl0.z & 0xf0u) << 4) | ((pl0.w & 0xf0u) << 8);
      const unsigned other = __builtin_amdgcn_mov_dpp(half16, 0xB1, 0xf, 0xf, true);
      const float lse = __uint_as_float((lane & 1) ? (other | (half16 << 16)) : (half16 | (other << 16)));
      float M = fmaxf(lse, dpp<ROW_ROR + 8>(lse)); M = xrow_max(M);
      const float wt_ = __builtin_amdgcn_exp2f(lse - M);
      float W = wt_; W += dpp<ROW_ROR + 8>(W); W = xrow_sum(W);
      float u[4];
#pragma unroll
      for (int i = 0; i < 4; ++i) u[i] = swap32_add(wt_ * unemb(pl0[i]), wt_ * unemb(pl1[i]));
      u[0] = swap16_add(u[0], u[2]); u[1] = swap16_add(u[1], u[3]);
      u[0] += dpp<ROW_ROR + 8>(u[0]); u[1] += dpp<ROW_ROR + 8>(u[1]);
      const float inv = gate * __builtin_amdgcn_rcpf(W);
      if ((lane & 8) == 0) *(LAS unsigned*)(L + L_O + (v * 64 + (lane >> 5) * 32 + (lane & 7) * 4 + ((lane >> 4) & 1) * 2) * 2) = pk_bf16(u[0] * inv, u[1] * inv);
      LGKM0();
      (void)lds_inc(L + L_FLAG2 + 4, lane);
      STAMP(1);
    }
    if ((r & 3) == 0) {
      const int R = (r >> 2) * 8 + kvh;
      const unsigned fo = WS_FG + (unsigned)(R * 8192 + v * 2048 + lane * 16);
      v4u pl0 = {0, 0, 0, 0}, pl1 = {0, 0, 0, 0};
      bool ok0 = false, ok1 = false;
      auto chk2 = [&](const v4u& x, const v4u& y, bool l0, bool l1) -> bool {
        const bool t0_ = l0 && emb_ok(x, t8), t1_ = l1 && emb_ok(y, t8);
#pragma unroll
        for (int i = 0; i < 4; ++i) { pl0[i] = t0_ ? x[i] : pl0[i]; pl1[i] = t1_ ? y[i] : pl1[i]; }
        ok0 = ok0 || t0_; ok1 = ok1 || t1_;
        return __builtin_amdgcn_ballot_w64(!(ok0 && ok1)) == 0ull;
      };
      for (unsigned spin = 0; spin < SPIN_CAP; ++spin) {
        CB();
        const bool l0 = !ok0, l1 = !ok1;
        const v4u c0 = __builtin_amdgcn_raw_buffer_load_b128(rws, l0 ? fo : OOB, 0, AUX_SC1);
        const v4u c1 = __builtin_amdgcn_raw_buffer_load_b128(rws, l1 ? fo + 1024 : OOB, 0, AUX_SC1);
        SB();
        if (chk2(c0, c1, l0, l1)) break;
      }
      float v0 = unemb(pl0.x) + unemb(pl1.x), v1 = unemb(pl0.y) + unemb(pl1.y), v2 = unemb(pl0.z) + unemb(pl1.z), v3 = unemb(pl0.w) + unemb(pl1.w);
      v0 = swap32_add(v0, v2); v1 = swap32_add(v1, v3);
      v0 = swap16_add(v0, v0); v1 = swap16_add(v1, v1);
      v0 += dpp<ROW_ROR + 8>(v0); v1 += dpp<ROW_ROR + 8>(v1);
      if ((lane & 24) == 0) *(LAS f32x2*)(L + L_RED + (v * 32 + (lane & 7) * 4 + (lane >> 5) * 2) * 4) = f32x2{v0, v1};
      LGKM0();
      const unsigned old = lds_inc(L + L_FLAG2 + 8, lane);
      CB();
      if (old == 3 && lane < 16) {
        const LAS f32x2* fr = (const LAS f32x2*)(L + L_RED) + lane;
        const f32x2 s = (fr[0] + fr[16]) + (fr[32] + fr[48]);
        __builtin_amdgcn_raw_buffer_store_b32(pk_bf16(s.x, s.y), mk(pout, 4096), (R * 16 + lane) * 4, 0, 0);
        __builtin_amdgcn_raw_buffer_store_b32(tag, rws, (blk == 0 && lane == 0) ? 0u : OOB, 0, AUX_SC1);
      }
    }
  }
  STAMP(7);
}
}

extern "C" void kernel_launch(void* const* d_in, const int* in_sizes, int n_in, void* d_out, int out_size, void* d_ws,
                              size_t ws_size, hipStream_t stream) {
  fk::fused_kernel<<<256, 512, 0, stream>>>(d_in[0], d_in[1], d_in[2], d_in[3], d_in[4], d_in[6], d_in[7], d_in[8], d_in[9], d_out, d_ws);
}
```

```cpp
#include <hip/hip_runtime.h>
#include <cstdint>

typedef unsigned v4u __attribute__((ext_vector_type(4)));
typedef unsigned v2u __attribute__((ext_vector_type(2)));
typedef float f32x4 __attribute__((ext_vector_type(4)));
typedef float f32x2 __attribute__((ext_vector_type(2)));
typedef short s16x4 __attribute__((ext_vector_type(4)));
typedef __bf16 bf16x8 __attribute__((ext_vector_type(8)));
typedef __bf16 bf16x2 __attribute__((ext_vector_type(2)));
typedef __amdgpu_buffer_rsrc_t rsrc_t;
#define LAS __attribute__((address_space(3)))

namespace fk {
constexpr int NBUF = 4;
constexpr int QPRE = 3;
constexpr int AUX_NT = 2, AUX_SC1 = 16;
constexpr unsigned OOB = 0x40000000u;
constexpr unsigned WS_QG = 4096;
constexpr unsigned WS_GU = WS_QG + 131072;
constexpr unsigned WS_PG = WS_GU + 114688;
constexpr unsigned WS_FG = WS_PG + 524288;
constexpr unsigned WS_END = WS_FG + 524288;
constexpr unsigned SPIN_CAP = 1u << 17;
constexpr int L_RED = 0;
constexpr int L_Q = 8192;
constexpr int L_H = L_Q + 1024;
constexpr int L_GATE = L_H + 256;
constexpr int L_FLAG = L_GATE + 16;
constexpr int L_FLAG2 = L_FLAG + 16;
constexpr int L_O = L_FLAG2 + 16;
constexpr int L_ML = L_O + 512;
constexpr int L_MRG = L_ML + 256;
constexpr int L_END = L_MRG + 16384;

__device__ __forceinline__ rsrc_t mk(const void* p, unsigned bytes) {
  return __builtin_amdgcn_make_buffer_rsrc((void*)p, 0, (int)bytes, 0x00020000);
}
__device__ __forceinline__ float dot2(unsigned a, unsigned b, float c) {
  return __builtin_amdgcn_fdot2_f32_bf16(__builtin_bit_cast(bf16x2, a), __builtin_bit_cast(bf16x2, b), c, false);
}
__device__ __forceinline__ unsigned pk_bf16(float lo, float hi) {
  f32x2 v = {lo, hi};
  return __builtin_bit_cast(unsigned, __builtin_convertvector(v, bf16x2));
}
__device__ __forceinline__ float bf_lo(unsigned u) { return __uint_as_float(u << 16); }
__device__ __forceinline__ float bf_hi(unsigned u) { return __uint_as_float(u & 0xffff0000u); }
template <int CTRL> __device__ __forceinline__ float dpp(float x) {
  return __builtin_bit_cast(float, __builtin_amdgcn_mov_dpp(__builtin_bit_cast(int, x), CTRL, 0xf, 0xf, true));
}
__device__ __forceinline__ float swap32_add(float a, float b) {
  auto s = __builtin_amdgcn_permlane32_swap(__float_as_uint(a), __float_as_uint(b), false, false);
  return __uint_as_float(s[0]) + __uint_as_float(s[1]);
}
__device__ __forceinline__ float swap16_add(float a, float b) {
  auto s = __builtin_amdgcn_permlane16_swap(__float_as_uint(a), __float_as_uint(b), false, false);
  return __uint_as_float(s[0]) + __uint_as_float(s[1]);
}
__device__ __forceinline__ float xrow_max(float x) {
  auto s = __builtin_amdgcn_permlane16_swap(__float_as_uint(x), __float_as_uint(x), false, false);
  x = fmaxf(__uint_as_float(s[0]), __uint_as_float(s[1]));
  auto t = __builtin_amdgcn_permlane32_swap(__float_as_uint(x), __float_as_uint(x), false, false);
  return fmaxf(__uint_as_float(t[0]), __uint_as_float(t[1]));
}
__device__ __forceinline__ float xrow_sum(float x) { x = swap16_add(x, x); return swap32_add(x, x); }
constexpr int ROW_ROR = 0x120;
__device__ __forceinline__ f32x4 treduce8(float (&v)[32], int lane) {
#pragma unroll
  for (int i = 0; i < 16; ++i) v[i] = swap32_add(v[i], v[i + 16]);
#pragma unroll
  for (int i = 0; i < 8; ++i) v[i] = swap16_add(v[i], v[i + 8]);
#pragma unroll
  for (int i = 0; i < 4; ++i) { float t1 = v[i] + dpp<ROW_ROR + 8>(v[i]), t2 = v[i + 4] + dpp<ROW_ROR + 8>(v[i + 4]); v[i] = (lane & 8) ? t2 : t1; }
  return f32x4{v[0], v[1], v[2], v[3]};
}
template <bool FIRST>
__device__ __forceinline__ void rowpair(float (&v)[32], const v4u& w0, const v4u& w1, unsigned x0, unsigned x1, unsigned x2, unsigned x3) {
  const unsigned xp[4] = {x0, x1, x2, x3};
#pragma unroll
  for (int d = 0; d < 4; ++d) {
    const unsigned lo = __builtin_amdgcn_perm(w1[d], w0[d], 0x05040100u), hi = __builtin_amdgcn_perm(w1[d], w0[d], 0x07060302u);
#pragma unroll
    for (int b = 0; b < 4; ++b) {
      v[b * 8 + 2 * d] = dot2(lo, xp[b], FIRST ? 0.f : v[b * 8 + 2 * d]);
      v[b * 8 + 2 * d + 1] = dot2(hi, xp[b], FIRST ? 0.f : v[b * 8 + 2 * d + 1]);
    }
  }
}
__device__ __forceinline__ f32x4 mfma32(v4u a, v4u b, f32x4 c) {
  return __builtin_amdgcn_mfma_f32_16x16x32_bf16(__builtin_bit_cast(bf16x8, a), __builtin_bit_cast(bf16x8, b), c, 0, 0, 0);
}
__device__ __forceinline__ f32x4 mfma16(v2u a, v2u b, f32x4 c) {
  return __builtin_amdgcn_mfma_f32_16x16x16bf16_1k(__builtin_bit_cast(s16x4, a), __builtin_bit_cast(s16x4, b), c, 0, 0, 0);
}
__device__ __forceinline__ unsigned lds_inc(LAS unsigned char* p, int lane) {
  unsigned old = 0;
  if (lane == 0) old = __hip_atomic_fetch_add((LAS unsigned*)p, 1u, __ATOMIC_RELAXED, __HIP_MEMORY_SCOPE_WORKGROUP);
  return __builtin_amdgcn_readfirstlane(old);
}
__device__ __forceinline__ unsigned emb(float v, unsigned low8) { return ((__float_as_uint(v) + 0x80u) & 0xffffff00u) | low8; }
__device__ __forceinline__ unsigned emb_bad(const v4u& a, unsigned t8) { return ((a.x ^ t8) | (a.y ^ t8) | (a.z ^ t8) | (a.w ^ t8)) & 0xffu; }
__device__ __forceinline__ float unemb(unsigned u) { return __uint_as_float(u & 0xffffff00u); }
__device__ __forceinline__ bool all8(unsigned long long ballot, int lane) { return ((unsigned)(ballot >> (lane & 56)) & 0xffu) == 0xffu; }
__device__ __forceinline__ void tr8x4(const v2u (&r)[8], v4u (&f)[4]) {
#pragma unroll
  for (int i = 0; i < 4; ++i) {
    f[0][i] = __builtin_amdgcn_perm(r[2 * i + 1].x, r[2 * i].x, 0x05040100u); f[1][i] = __builtin_amdgcn_perm(r[2 * i + 1].x, r[2 * i].x, 0x07060302u);
    f[2][i] = __builtin_amdgcn_perm(r[2 * i + 1].y, r[2 * i].y, 0x05040100u); f[3][i] = __builtin_amdgcn_perm(r[2 * i + 1].y, r[2 * i].y, 0x07060302u);
  }
}
#define SB() __builtin_amdgcn_sched_barrier(0)
#define CB() asm volatile("" ::: "memory")
#define LGKM0() asm volatile("s_waitcnt lgkmcnt(0)" ::: "memory")

__global__ void __launch_bounds__(512, 2)
fused_kernel(const void* __restrict__ px, const void* __restrict__ pk, const void* __restrict__ pv, const void* __restrict__ pwq,
             const void* __restrict__ pwo, const void* __restrict__ pwg, const void* __restrict__ pwu, const void* __restrict__ pwd,
             const void* __restrict__ pgw, void* __restrict__ pout, void* pws) {
  __shared__ __attribute__((aligned(16))) unsigned char smem[L_END];
  LAS unsigned char* L = (LAS unsigned char*)smem;
  asm volatile("" :: "s"(px), "s"(pk), "s"(pv), "s"(pwq), "s"(pwo), "s"(pwg), "s"(pwu), "s"(pwd), "s"(pgw), "s"(pout), "s"(pws));
  const int tid = threadIdx.x, lane = tid & 63, w = __builtin_amdgcn_readfirstlane(tid >> 6);
  const int g = lane >> 4, c = lane & 15;
  const int blk = blockIdx.x, kvh = blk & 7, r = blk >> 3;
  const int b_att = r >> 3, s_att = r & 7;
  const int kv2 = (kvh + 4) & 7;
  const int rb = r >> 2, cq = r & 3;
  const int pidx = kv2 * 8 + rb;

  const size_t kvoff = ((size_t)(b_att * 8 + kvh) * 8192 + s_att * 1024) * 256;
  const rsrc_t rk = mk((const char*)pk + kvoff, 262144), rv = mk((const char*)pv + kvoff, 262144);
  const rsrc_t rws = mk(pws, WS_END);
  const rsrc_t rx = mk(px, 4096), rgw = mk(pgw, 1024);
  const rsrc_t rwo = mk(pwo, 4096 * 512 * 2), rwd = mk(pwd, 1792 * 512 * 2);

  __builtin_amdgcn_s_setprio(3);
  const bool isq = r < 16;
  const int gu = kvh * 14 + (r - 16);
  const int um = gu >= 56 ? 1 : 0;
  const int ulc = isq ? kvh * 8 + (r >> 1) : ((gu - 56 * um) >> 1);
  const int uh = isq ? (r & 1) : (gu & 1);
  v4u wt[4]; v2u xq[4];
  {
    const int row = uh * 256 + w * 32 + 4 * (lane >> 3);
    const unsigned rstr = isq ? 8192u : 3584u;
    const rsrc_t ru = mk(isq ? pwq : (um ? pwu : pwg), isq ? 512 * 4096 * 2 : 512 * 1792 * 2);
    const unsigned uo = r < 30 ? (unsigned)row * rstr + (unsigned)(ulc * 128 + (lane & 7) * 16) : OOB;
#pragma unroll
    for (int i = 0; i < 4; ++i) wt[i] = __builtin_amdgcn_raw_buffer_load_b128(ru, uo + i * rstr, 0, 0);
#pragma unroll
    for (int b = 0; b < 4; ++b) xq[b] = __builtin_amdgcn_raw_buffer_load_b64(rx, row * 2 + b * 1024, 0, 0);
  }
  SB();
  if (tid == 0) { *(volatile LAS v4u*)(L + L_FLAG) = v4u{0u, 0u, 0u, 0u}; *(volatile LAS v4u*)(L + L_FLAG2) = v4u{0u, 0u, 0u, 0u}; }
  LGKM0(); __builtin_amdgcn_s_barrier(); CB();
  SB();
  const unsigned ko = c * 256 + g * 16, vo = g * 1024 + c * 16;
  v4u kb[NBUF][4], vb[NBUF][4];
  auto issueK = [&](int j) {
    const unsigned so = (unsigned)(j * 8 + w) * 4096u;
#pragma unroll
    for (int i = 0; i < 4; ++i) kb[j % NBUF][i] = __builtin_amdgcn_raw_buffer_load_b128(rk, ko + i * 64, so, AUX_NT);
    SB();
  };
  auto issueV = [&](int j) {
    const unsigned so = (unsigned)(j * 8 + w) * 4096u;
#pragma unroll
    for (int i = 0; i < 4; ++i) vb[j % NBUF][i] = __builtin_amdgcn_raw_buffer_load_b128(rv, vo + i * 256, so, AUX_NT);
    SB();
  };
  auto issue = [&](int j) { issueK(j); issueV(j); };
  const unsigned E = *(const __attribute__((address_space(4))) unsigned*)pws;
  SB();
  issueV(0);
  issueK(0);
  const unsigned tag = E * 0x9E3779B5u + 0x7F4A7C15u;
  const unsigned t8 = tag & 0xffu;
  {
    LAS f32x4* red = (LAS f32x4*)(L + L_RED);
    float v[32];
    rowpair<true>(v, wt[0], wt[1], xq[0].x, xq[1].x, xq[2].x, xq[3].x);
    rowpair<false>(v, wt[2], wt[3], xq[0].y, xq[1].y, xq[2].y, xq[3].y);
    red[w * 64 + lane] = treduce8(v, lane);
    LGKM0();
    const unsigned old = lds_inc(L + L_FLAG + 4, lane);
    CB();
    const f32x4 s = ((red[lane] + red[64 + lane]) + (red[128 + lane] + red[192 + lane])) + ((red[256 + lane] + red[320 + lane]) + (red[384 + lane] + red[448 + lane]));
    const int c64 = (lane & 7) * 8 + ((lane >> 3) & 1) * 4;
    const unsigned po = isq ? WS_QG + (unsigned)((((kvh * 4 + g) * 2 + uh) * 512 + (r >> 1) * 64 + c64) * 4)
                            : WS_GU + (unsigned)((((um * 2 + uh) * 4 + g) * 1792 + ulc * 64 + c64) * 4);
    __builtin_amdgcn_raw_buffer_store_b128(v4u{emb(s[0], t8), emb(s[1], t8), emb(s[2], t8), emb(s[3], t8)}, rws, (old == 7 && r < 30) ? po : OOB, 0, AUX_SC1);
    SB();
    __builtin_amdgcn_s_setprio(0);
    SB();
  }
#pragma unroll
  for (int j = 1; j < QPRE; ++j) issue(j);
  const unsigned qgo = w < 2 ? WS_QG + (unsigned)((kvh * 4 + b_att) * 4096 + w * 1024 + lane * 16) : OOB;
  CB();
  v4u qa = __builtin_amdgcn_raw_buffer_load_b128(rws, qgo, 0, AUX_SC1);
  v4u qb = __builtin_amdgcn_raw_buffer_load_b128(rws, qgo + 2048, 0, AUX_SC1);
  SB();
  issue(QPRE);
  if (w < 2) {
    if (__builtin_amdgcn_ballot_w64((emb_bad(qa, t8) | emb_bad(qb, t8)) != 0u) != 0ull) {
      for (unsigned spin = 0; spin < SPIN_CAP; ++spin) {
        CB();
        qa = __builtin_amdgcn_raw_buffer_load_b128(rws, qgo, 0, AUX_SC1);
        qb = __builtin_amdgcn_raw_buffer_load_b128(rws, qgo + 2048, 0, AUX_SC1);
        if (__builtin_amdgcn_ballot_w64((emb_bad(qa, t8) | emb_bad(qb, t8)) != 0u) == 0ull) break;
      }
    }
    *(LAS v2u*)(L + L_Q + (w * 128 + lane * 2) * 4) = v2u{pk_bf16(unemb(qa.x) + unemb(qb.x), unemb(qa.y) + unemb(qb.y)), pk_bf16(unemb(qa.z) + unemb(qb.z), unemb(qa.w) + unemb(qb.w))};
    LGKM0();
    (void)lds_inc(L + L_FLAG, lane);
  }
  SB();
#pragma unroll
  for (int j = QPRE + 1; j < NBUF; ++j) issue(j);
  auto lds_wait = [&](int off, unsigned n) {
    for (unsigned spin = 0; spin < (SPIN_CAP << 4); ++spin) {
      if (*(volatile LAS unsigned*)(L + off) >= n) break;
      __builtin_amdgcn_s_sleep(1);
    }
    CB();
  };
  lds_wait(L_FLAG, 2u);
  v4u qf[4];
#pragma unroll
  for (int i = 0; i < 4; ++i) qf[i] = *(LAS v4u*)(L + L_Q + (((c & 3) * 64 + 16 * i + 4 * g) * 4));

  f32x4 acc[8];
#pragma unroll
  for (int e = 0; e < 8; ++e) acc[e] = f32x4{0.f, 0.f, 0.f, 0.f};
  float m = -INFINITY, l = 0.f;
#pragma unroll
  for (int j = 0; j < 8; ++j) {
    const int bf = j % NBUF;
    f32x4 sc = {0.f, 0.f, 0.f, 0.f};
#pragma unroll
    for (int i = 0; i < 4; ++i) sc = mfma32(kb[bf][i], qf[i], sc);
    constexpr float SCL = 0.08838834764831845f, L2E = 1.4426950408889634f;
    const unsigned s01 = pk_bf16(sc[0], sc[1]), s23 = pk_bf16(sc[2], sc[3]);
    const unsigned u01 = pk_bf16(bf_lo(s01) * SCL, bf_hi(s01) * SCL), u23 = pk_bf16(bf_lo(s23) * SCL, bf_hi(s23) * SCL);
    const float t0 = bf_lo(u01) * L2E, t1 = bf_hi(u01) * L2E, t2 = bf_lo(u23) * L2E, t3 = bf_hi(u23) * L2E;
    const float cm = xrow_max(fmaxf(fmaxf(t0, t1), fmaxf(t2, t3)));
    const float mn = fmaxf(m, cm);
    const float alpha = __builtin_amdgcn_exp2f(m - mn);
    const float p0 = __builtin_amdgcn_exp2f(t0 - mn), p1 = __builtin_amdgcn_exp2f(t1 - mn), p2 = __builtin_amdgcn_exp2f(t2 - mn), p3 = __builtin_amdgcn_exp2f(t3 - mn);
    l = l * alpha + ((p0 + p1) + (p2 + p3));
    m = mn;
    const v2u pa = {pk_bf16(p0, p1), pk_bf16(p2, p3)};
    if (j > 0) {
      const float a0 = dpp<0x00>(alpha), a1 = dpp<0x55>(alpha), a2 = dpp<0xAA>(alpha), a3 = dpp<0xFF>(alpha);
#pragma unroll
      for (int e = 0; e < 8; ++e) { acc[e][0] *= a0; acc[e][1] *= a1; acc[e][2] *= a2; acc[e][3] *= a3; }
    }
#pragma unroll
    for (int d = 0; d < 4; ++d) {
      const v2u lo = {__builtin_amdgcn_perm(vb[bf][1][d], vb[bf][0][d], 0x05040100u), __builtin_amdgcn_perm(vb[bf][3][d], vb[bf][2][d], 0x05040100u)};
      const v2u hi = {__builtin_amdgcn_perm(vb[bf][1][d], vb[bf][0][d], 0x07060302u), __builtin_amdgcn_perm(vb[bf][3][d], vb[bf][2][d], 0x07060302u)};
      acc[2 * d] = mfma16(pa, lo, acc[2 * d]);
      acc[2 * d + 1] = mfma16(pa, hi, acc[2 * d + 1]);
    }
    if (j == QPRE) asm volatile("" :: "v"(qa), "v"(qb));
    SB();
    if (j + NBUF < 8) issue(j + NBUF);
    if (j + NBUF == 7) { if (lane == 0) (void)__hip_atomic_fetch_add((LAS unsigned*)(L + L_FLAG + 8), 1u, __ATOMIC_RELAXED, __HIP_MEMORY_SCOPE_WORKGROUP); SB(); }
  }
  l = xrow_sum(l);
  if (g == 0) {
    LAS float* mo = (LAS float*)(L + L_MRG) + w * 512 + 8 * c;
#pragma unroll
    for (int h = 0; h < 4; ++h) {
      *(LAS f32x4*)(mo + h * 128) = f32x4{acc[0][h], acc[1][h], acc[2][h], acc[3][h]};
      *(LAS f32x4*)(mo + h * 128 + 4) = f32x4{acc[4][h], acc[5][h], acc[6][h], acc[7][h]};
    }
    if (c < 4) *(LAS f32x2*)(L + L_ML + (c * 8 + w) * 8) = f32x2{m, l};
  }
  LGKM0();
  (void)lds_inc(L + L_FLAG + 12, lane);
  const f32x4 zero = {0.f, 0.f, 0.f, 0.f};
  if (w < 2) {
    lds_wait(L_FLAG + 8, 8u);
    v2u r0[8], r1[8], r2[8];
    v4u bo[2][4], bd[4];
    const unsigned colb = (unsigned)(cq * 128 + w * 64 + 4 * c) * 2u;
    const unsigned wdb = (unsigned)(pidx * 28 + 8 * g) * 1024u + colb;
#pragma unroll
    for (int i = 0; i < 8; ++i) r2[i] = __builtin_amdgcn_raw_buffer_load_b64(rwd, (8 * g + i < 28) ? wdb + i * 1024 : OOB, 0, 0);
    const unsigned wob = (unsigned)(kv2 * 512 + rb * 64 + 8 * g) * 1024u + colb;
#pragma unroll
    for (int i = 0; i < 8; ++i) r0[i] = __builtin_amdgcn_raw_buffer_load_b64(rwo, wob + i * 1024, 0, 0);
#pragma unroll
    for (int i = 0; i < 8; ++i) r1[i] = __builtin_amdgcn_raw_buffer_load_b64(rwo, wob + 32768 + i * 1024, 0, 0);
    SB();
    unsigned fgo = WS_FG + (unsigned)((((g * 16 + cq * 4 + w * 2 + (c >> 3)) * 64 + pidx) * 32 + (c & 7) * 4) * 4);
    lds_wait(L_FLAG2, 1u);
    const v4u hf = *(LAS v4u*)(L + L_H + ((c >> 2) * 32 + 8 * g) * 2);
    tr8x4(r2, bd);
    f32x4 db[4];
#pragma unroll
    for (int e = 0; e < 4; ++e) db[e] = mfma32(hf, bd[e], zero);
    tr8x4(r0, bo[0]); tr8x4(r1, bo[1]);
#pragma unroll
    for (int e = 0; e < 4; ++e) asm volatile("" : "+v"(bo[0][e]), "+v"(bo[1][e]), "+v"(db[e]));
    asm volatile("" : "+v"(fgo));
    lds_wait(L_FLAG2 + 4, 4u);
    const v4u of0 = *(LAS v4u*)(L + L_O + ((c >> 2) * 64 + 8 * g) * 2), of1 = *(LAS v4u*)(L + L_O + ((c >> 2) * 64 + 32 + 8 * g) * 2);
#pragma unroll
    for (int e = 0; e < 4; ++e) db[e] = mfma32(of0, bo[0][e], db[e]);
#pragma unroll
    for (int e = 0; e < 4; ++e) db[e] = mfma32(of1, bo[1][e], db[e]);
    __builtin_amdgcn_raw_buffer_store_b128(v4u{emb(db[0][0], t8), emb(db[1][0], t8), emb(db[2][0], t8), emb(db[3][0], t8)}, rws, fgo, 0, AUX_SC1);
  } else if (w == 2) {
    const unsigned go = c < 7 ? WS_GU + (unsigned)((g * 1792 + pidx * 28 + c * 4) * 4) : OOB;
    v4u xv[4];
#pragma unroll
    for (int b = 0; b < 4; ++b) xv[b] = __builtin_amdgcn_raw_buffer_load_b128(rx, lane * 16 + b * 1024, 0, 0);
    const v4u gv = __builtin_amdgcn_raw_buffer_load_b128(rgw, lane * 16, 0, 0);
    v4u gq[4];
    for (unsigned spin = 0; spin < SPIN_CAP; ++spin) {
      CB();
#pragma unroll
      for (int i = 0; i < 4; ++i) gq[i] = __builtin_amdgcn_raw_buffer_load_b128(rws, go + i * 28672, 0, AUX_SC1);
      SB();
      if (__builtin_amdgcn_ballot_w64(c >= 7 || (emb_bad(gq[0], t8) | emb_bad(gq[1], t8) | emb_bad(gq[2], t8) | emb_bad(gq[3], t8)) == 0u) == ~0ull) break;
      __builtin_amdgcn_s_sleep(2);
    }
    float a[4];
#pragma unroll
    for (int b = 0; b < 4; ++b) { a[b] = dot2(xv[b].x, gv.x, 0.f); a[b] = dot2(xv[b].y, gv.y, a[b]); a[b] = dot2(xv[b].z, gv.z, a[b]); a[b] = dot2(xv[b].w, gv.w, a[b]); }
    a[0] = swap32_add(a[0], a[2]); a[1] = swap32_add(a[1], a[3]);
    a[0] = swap16_add(a[0], a[1]);
    a[0] += dpp<ROW_ROR + 8>(a[0]); a[0] += dpp<ROW_ROR + 4>(a[0]); a[0] += dpp<ROW_ROR + 2>(a[0]); a[0] += dpp<ROW_ROR + 1>(a[0]);
    const float gt = 1.f / (1.f + __expf(-a[0]));
    if (c == 0) *(LAS float*)(L + L_GATE + g * 4) = gt;
    float hh[4];
#pragma unroll
    for (int i = 0; i < 4; ++i) hh[i] = (1.f - gt) * (unemb(gq[2][i]) + unemb(gq[3][i])) / (1.f + __expf(-(unemb(gq[0][i]) + unemb(gq[1][i]))));
    if (c < 8) *(LAS v2u*)(L + L_H + (g * 16 + c * 2) * 4) = c < 7 ? v2u{pk_bf16(hh[0], hh[1]), pk_bf16(hh[2], hh[3])} : v2u{0u, 0u};
    LGKM0();
    if (lane == 0) *(volatile LAS unsigned*)(L + L_FLAG2) = 1u;
  } else if (w >= 4) {
    const int v = w - 4;
    unsigned pgo = WS_PG + (unsigned)(((kvh * 32 + r) * 512 + v * 128 + lane * 2) * 4);
    unsigned po = WS_PG + (unsigned)(((kv2 * 32 + v * 8 + (lane >> 3)) * 512 + rb * 64 + (lane & 7) * 4) * 4);
    asm volatile("" : "+v"(pgo), "+v"(po));
    lds_wait(L_FLAG + 12, 8u);
    {
      f32x4 ml[4]; f32x2 ov[8];
#pragma unroll
      for (int i = 0; i < 4; ++i) ml[i] = *(LAS f32x4*)(L + L_ML + v * 64 + i * 16);
#pragma unroll
      for (int i = 0; i < 8; ++i) ov[i] = *(LAS f32x2*)(L + L_MRG + (i * 512 + v * 128 + lane * 2) * 4);
      SB();
      const float M = fmaxf(fmaxf(fmaxf(ml[0][0], ml[0][2]), fmaxf(ml[1][0], ml[1][2])), fmaxf(fmaxf(ml[2][0], ml[2][2]), fmaxf(ml[3][0], ml[3][2])));
      float Ls = 0.f; f32x2 o = {0.f, 0.f};
#pragma unroll
      for (int i = 0; i < 4; ++i) {
        const float w0 = __builtin_amdgcn_exp2f(ml[i][0] - M), w1 = __builtin_amdgcn_exp2f(ml[i][2] - M);
        Ls += w0 * ml[i][1] + w1 * ml[i][3]; o += w0 * ov[2 * i] + w1 * ov[2 * i + 1];
      }
      o *= __builtin_amdgcn_rcpf(Ls);
      const unsigned lseu = __float_as_uint(M + __builtin_amdgcn_logf(Ls));
      const unsigned nb2 = lseu >> ((lane & 3) * 8);
      const unsigned t4 = t8 & 0x0fu;
      __builtin_amdgcn_raw_buffer_store_b64(v2u{emb(o.x, t4 | ((nb2 & 15u) << 4)), emb(o.y, t4 | (nb2 & 0xf0u))}, rws, pgo, 0, AUX_SC1);
      SB();
    }
    {
      v4u pl0 = {0, 0, 0, 0}, pl1 = {0, 0, 0, 0};
      bool pok = false;
      lds_wait(L_FLAG2, 1u);
      const float gate = *(LAS float*)(L + L_GATE + v * 4);
      for (unsigned spin = 0; spin < SPIN_CAP; ++spin) {
        CB();
        const bool lv = !pok;
        const unsigned o_ = lv ? po : OOB;
        const v4u a0 = __builtin_amdgcn_raw_buffer_load_b128(rws, o_, 0, AUX_SC1);
        const v4u a1 = __builtin_amdgcn_raw_buffer_load_b128(rws, o_ + 128, 0, AUX_SC1);
        SB();
        const bool take = all8(__builtin_amdgcn_ballot_w64(lv && ((emb_bad(a0, t8) | emb_bad(a1, t8)) & 0x0fu) == 0u), lane);
#pragma unroll
        for (int i = 0; i < 4; ++i) { pl0[i] = take ? a0[i] : pl0[i]; pl1[i] = take ? a1[i] : pl1[i]; }
        pok = pok || take;
        if (__builtin_amdgcn_ballot_w64(!pok) == 0ull) break;
      }
      const unsigned half16 = ((pl0.x >> 4) & 15u) | (pl0.y & 0xf0u) | ((pl0.z & 0xf0u) << 4) | ((pl0.w & 0xf0u) << 8);
      const unsigned other = __builtin_amdgcn_mov_dpp(half16, 0xB1, 0xf, 0xf, true);
      const float lse = __uint_as_float((lane & 1) ? (other | (half16 << 16)) : (half16 | (other << 16)));
      float M = fmaxf(lse, dpp<ROW_ROR + 8>(lse)); M = xrow_max(M);
      const float wt_ = __builtin_amdgcn_exp2f(lse - M);
      float W = wt_; W += dpp<ROW_ROR + 8>(W); W = xrow_sum(W);
      float u[4];
#pragma unroll
      for (int i = 0; i < 4; ++i) u[i] = swap32_add(wt_ * unemb(pl0[i]), wt_ * unemb(pl1[i]));
      u[0] = swap16_add(u[0], u[2]); u[1] = swap16_add(u[1], u[3]);
      u[0] += dpp<ROW_ROR + 8>(u[0]); u[1] += dpp<ROW_ROR + 8>(u[1]);
      const float inv = gate * __builtin_amdgcn_rcpf(W);
      if ((lane & 8) == 0) *(LAS unsigned*)(L + L_O + (v * 64 + (lane >> 5) * 32 + (lane & 7) * 4 + ((lane >> 4) & 1) * 2) * 2) = pk_bf16(u[0] * inv, u[1] * inv);
      LGKM0();
      (void)lds_inc(L + L_FLAG2 + 4, lane);
    }
    if ((r & 3) == 0) {
      const int R = (r >> 2) * 8 + kvh;
      const unsigned fo = WS_FG + (unsigned)(R * 8192 + v * 2048 + lane * 16);
      v4u pl0 = {0, 0, 0, 0}, pl1 = {0, 0, 0, 0};
      bool ok0 = false, ok1 = false;
      for (unsigned spin = 0; spin < SPIN_CAP; ++spin) {
        CB();
        const bool l0 = !ok0, l1 = !ok1;
        const v4u c0 = __builtin_amdgcn_raw_buffer_load_b128(rws, l0 ? fo : OOB, 0, AUX_SC1);
        const v4u c1 = __builtin_amdgcn_raw_buffer_load_b128(rws, l1 ? fo + 1024 : OOB, 0, AUX_SC1);
        SB();
        const bool t0_ = all8(__builtin_amdgcn_ballot_w64(l0 && emb_bad(c0, t8) == 0u), lane);
        const bool t1_ = all8(__builtin_amdgcn_ballot_w64(l1 && emb_bad(c1, t8) == 0u), lane);
#pragma unroll
        for (int i = 0; i < 4; ++i) { pl0[i] = t0_ ? c0[i] : pl0[i]; pl1[i] = t1_ ? c1[i] : pl1[i]; }
        ok0 = ok0 || t0_; ok1 = ok1 || t1_;
        if (__builtin_amdgcn_ballot_w64(!(ok0 && ok1)) == 0ull) break;
      }
      float v0 = unemb(pl0.x) + unemb(pl1.x), v1 = unemb(pl0.y) + unemb(pl1.y), v2 = unemb(pl0.z) + unemb(pl1.z), v3 = unemb(pl0.w) + unemb(pl1.w);
      v0 = swap32_add(v0, v2); v1 = swap32_add(v1, v3);
      v0 = swap16_add(v0, v0); v1 = swap16_add(v1, v1);
      v0 += dpp<ROW_ROR + 8>(v0); v1 += dpp<ROW_ROR + 8>(v1);
      if ((lane & 24) == 0) *(LAS f32x2*)(L + L_RED + (v * 32 + (lane & 7) * 4 + (lane >> 5) * 2) * 4) = f32x2{v0, v1};
      LGKM0();
      const unsigned old = lds_inc(L + L_FLAG2 + 8, lane);
      CB();
      if (old == 3 && lane < 16) {
        const LAS f32x2* fr = (const LAS f32x2*)(L + L_RED) + lane;
        const f32x2 s = (fr[0] + fr[16]) + (fr[32] + fr[48]);
        __builtin_amdgcn_raw_buffer_store_b32(pk_bf16(s.x, s.y), mk(pout, 4096), (R * 16 + lane) * 4, 0, 0);
        __builtin_amdgcn_raw_buffer_store_b32(tag, rws, (blk == 0 && lane == 0) ? 0u : OOB, 0, AUX_SC1);
      }
    }
  }
}
}

extern "C" void kernel_launch(void* const* d_in, const int* in_sizes, int n_in, void* d_out, int out_size, void* d_ws,
                              size_t ws_size, hipStream_t stream) {
  fk::fused_kernel<<<256, 512, 0, stream>>>(d_in[0], d_in[1], d_in[2], d_in[3], d_in[4], d_in[6], d_in[7], d_in[8], d_in[9], d_out, d_ws);
}
```

```cpp
#include <hip/hip_runtime.h>
#include <cstdint>

typedef unsigned v4u __attribute__((ext_vector_type(4)));
typedef unsigned v2u __attribute__((ext_vector_type(2)));
typedef float f32x4 __attribute__((ext_vector_type(4)));
typedef float f32x2 __attribute__((ext_vector_type(2)));
typedef short s16x4 __attribute__((ext_vector_type(4)));
typedef __bf16 bf16x8 __attribute__((ext_vector_type(8)));
typedef __bf16 bf16x2 __attribute__((ext_vector_type(2)));
typedef __amdgpu_buffer_rsrc_t rsrc_t;
#define LAS __attribute__((address_space(3)))

namespace fk {
constexpr int NBUF = 4;
constexpr int QPRE = 3;
constexpr int AUX_NT = 2, AUX_SC1 = 16;
constexpr unsigned OOB = 0x40000000u;
constexpr unsigned WS_QG = 4096;
constexpr unsigned WS_GU = WS_QG + 131072;
constexpr unsigned WS_PG = WS_GU + 114688;
constexpr unsigned WS_FG = WS_PG + 524288;
constexpr unsigned WS_END = WS_FG + 524288;
constexpr unsigned SPIN_CAP = 1u << 17;
constexpr int L_RED = 0;
constexpr int L_Q = 8192;
constexpr int L_H = L_Q + 1024;
constexpr int L_GATE = L_H + 256;
constexpr int L_FLAG = L_GATE + 16;
constexpr int L_FLAG2 = L_FLAG + 16;
constexpr int L_O = L_FLAG2 + 16;
constexpr int L_ML = L_O + 512;
constexpr int L_MRG = L_ML + 256;
constexpr int L_END = L_MRG + 16384;

__device__ __forceinline__ rsrc_t mk(const void* p, unsigned bytes) {
  return __builtin_amdgcn_make_buffer_rsrc((void*)p, 0, (int)bytes, 0x00020000);
}
__device__ __forceinline__ float dot2(unsigned a, unsigned b, float c) {
  return __builtin_amdgcn_fdot2_f32_bf16(__builtin_bit_cast(bf16x2, a), __builtin_bit_cast(bf16x2, b), c, false);
}
__device__ __forceinline__ unsigned pk_bf16(float lo, float hi) {
  f32x2 v = {lo, hi};
  return __builtin_bit_cast(unsigned, __builtin_convertvector(v, bf16x2));
}
__device__ __forceinline__ float bf_lo(unsigned u) { return __uint_as_float(u << 16); }
__device__ __forceinline__ float bf_hi(unsigned u) { return __uint_as_float(u & 0xffff0000u); }
template <int CTRL> __device__ __forceinline__ float dpp(float x) {
  return __builtin_bit_cast(float, __builtin_amdgcn_mov_dpp(__builtin_bit_cast(int, x), CTRL, 0xf, 0xf, true));
}
__device__ __forceinline__ float swap32_add(float a, float b) {
  auto s = __builtin_amdgcn_permlane32_swap(__float_as_uint(a), __float_as_uint(b), false, false);
  return __uint_as_float(s[0]) + __uint_as_float(s[1]);
}
__device__ __forceinline__ float swap16_add(float a, float b) {
  auto s = __builtin_amdgcn_permlane16_swap(__float_as_uint(a), __float_as_uint(b), false, false);
  return __uint_as_float(s[0]) + __uint_as_float(s[1]);
}
__device__ __forceinline__ float xrow_max(float x) {
  auto s = __builtin_amdgcn_permlane16_swap(__float_as_uint(x), __float_as_uint(x), false, false);
  x = fmaxf(__uint_as_float(s[0]), __uint_as_float(s[1]));
  auto t = __builtin_amdgcn_permlane32_swap(__float_as_uint(x), __float_as_uint(x), false, false);
  return fmaxf(__uint_as_float(t[0]), __uint_as_float(t[1]));
}
__device__ __forceinline__ float xrow_sum(float x) { x = swap16_add(x, x); return swap32_add(x, x); }
constexpr int ROW_ROR = 0x120;
__device__ __forceinline__ f32x4 treduce8(float (&v)[32], int lane) {
#pragma unroll
  for (int i = 0; i < 16; ++i) v[i] = swap32_add(v[i], v[i + 16]);
#pragma unroll
  for (int i = 0; i < 8; ++i) v[i] = swap16_add(v[i], v[i + 8]);
#pragma unroll
  for (int i = 0; i < 4; ++i) { float t1 = v[i] + dpp<ROW_ROR + 8>(v[i]), t2 = v[i + 4] + dpp<ROW_ROR + 8>(v[i + 4]); v[i] = (lane & 8) ? t2 : t1; }
  return f32x4{v[0], v[1], v[2], v[3]};
}
template <bool FIRST>
__device__ __forceinline__ void rowpair(float (&v)[32], const v4u& w0, const v4u& w1, unsigned x0, unsigned x1, unsigned x2, unsigned x3) {
  const unsigned xp[4] = {x0, x1, x2, x3};
#pragma unroll
  for (int d = 0; d < 4; ++d) {
    const unsigned lo = __builtin_amdgcn_perm(w1[d], w0[d], 0x05040100u), hi = __builtin_amdgcn_perm(w1[d], w0[d], 0x07060302u);
#pragma unroll
    for (int b = 0; b < 4; ++b) {
      v[b * 8 + 2 * d] = dot2(lo, xp[b], FIRST ? 0.f : v[b * 8 + 2 * d]);
      v[b * 8 + 2 * d + 1] = dot2(hi, xp[b], FIRST ? 0.f : v[b * 8 + 2 * d + 1]);
    }
  }
}
__device__ __forceinline__ f32x4 mfma32(v4u a, v4u b, f32x4 c) {
  return __builtin_amdgcn_mfma_f32_16x16x32_bf16(__builtin_bit_cast(bf16x8, a), __builtin_bit_cast(bf16x8, b), c, 0, 0, 0);
}
__device__ __forceinline__ f32x4 mfma16(v2u a, v2u b, f32x4 c) {
  return __builtin_amdgcn_mfma_f32_16x16x16bf16_1k(__builtin_bit_cast(s16x4, a), __builtin_bit_cast(s16x4, b), c, 0, 0, 0);
}
__device__ __forceinline__ unsigned lds_inc(LAS unsigned char* p, int lane) {
  unsigned old = 0;
  if (lane == 0) old = __hip_atomic_fetch_add((LAS unsigned*)p, 1u, __ATOMIC_RELAXED, __HIP_MEMORY_SCOPE_WORKGROUP);
  return __builtin_amdgcn_readfirstlane(old);
}
__device__ __forceinline__ unsigned emb(float v, unsigned low8) { return ((__float_as_uint(v) + 0x80u) & 0xffffff00u) | low8; }
__device__ __forceinline__ unsigned emb_bad(const v4u& a, unsigned t8) { return ((a.x ^ t8) | (a.y ^ t8) | (a.z ^ t8) | (a.w ^ t8)) & 0xffu; }
__device__ __forceinline__ float unemb(unsigned u) { return __uint_as_float(u & 0xffffff00u); }
__device__ __forceinline__ bool all8(unsigned long long ballot, int lane) { return ((unsigned)(ballot >> (lane & 56)) & 0xffu) == 0xffu; }
__device__ __forceinline__ bool all2(unsigned long long ballot, int lane) { return ((unsigned)(ballot >> (lane & 62)) & 3u) == 3u; }
__device__ __forceinline__ void tr8x4(const v2u (&r)[8], v4u (&f)[4]) {
#pragma unroll
  for (int i = 0; i < 4; ++i) {
    f[0][i] = __builtin_amdgcn_perm(r[2 * i + 1].x, r[2 * i].x, 0x05040100u); f[1][i] = __builtin_amdgcn_perm(r[2 * i + 1].x, r[2 * i].x, 0x07060302u);
    f[2][i] = __builtin_amdgcn_perm(r[2 * i + 1].y, r[2 * i].y, 0x05040100u); f[3][i] = __builtin_amdgcn_perm(r[2 * i + 1].y, r[2 * i].y, 0x07060302u);
  }
}
#define SB() __builtin_amdgcn_sched_barrier(0)
#define CB() asm volatile("" ::: "memory")
#define LGKM0() asm volatile("s_waitcnt lgkmcnt(0)" ::: "memory")

__global__ void __launch_bounds__(512, 2)
fused_kernel(const void* __restrict__ px, const void* __restrict__ pk, const void* __restrict__ pv, const void* __restrict__ pwq,
             const void* __restrict__ pwo, const void* __restrict__ pwg, const void* __restrict__ pwu, const void* __restrict__ pwd,
             const void* __restrict__ pgw, void* __restrict__ pout, void* pws) {
  __shared__ __attribute__((aligned(16))) unsigned char smem[L_END];
  LAS unsigned char* L = (LAS unsigned char*)smem;
  asm volatile("" :: "s"(px), "s"(pk), "s"(pv), "s"(pwq), "s"(pwo), "s"(pwg), "s"(pwu), "s"(pwd), "s"(pgw), "s"(pout), "s"(pws));
  const int tid = threadIdx.x, lane = tid & 63, w = __builtin_amdgcn_readfirstlane(tid >> 6);
  const int g = lane >> 4, c = lane & 15;
  const int blk = blockIdx.x, kvh = blk & 7, r = blk >> 3;
  const int b_att = r >> 3, s_att = r & 7;
  const int kv2 = (kvh + 4) & 7;
  const int rb = r >> 2, cq = r & 3;
  const int pidx = kv2 * 8 + rb;

  const size_t kvoff = ((size_t)(b_att * 8 + kvh) * 8192 + s_att * 1024) * 256;
  const rsrc_t rk = mk((const char*)pk + kvoff, 262144), rv = mk((const char*)pv + kvoff, 262144);
  const rsrc_t rws = mk(pws, WS_END);
  const rsrc_t rx = mk(px, 4096), rgw = mk(pgw, 1024);
  const rsrc_t rwo = mk(pwo, 4096 * 512 * 2), rwd = mk(pwd, 1792 * 512 * 2);

  __builtin_amdgcn_s_setprio(3);
  const bool isq = r < 16;
  const int gu = kvh * 14 + (r - 16);
  const int um = gu >= 56 ? 1 : 0;
  const int ulc = isq ? kvh * 8 + (r >> 1) : ((gu - 56 * um) >> 1);
  const int uh = isq ? (r & 1) : (gu & 1);
  v4u wt[4]; v2u xq[4];
  {
    const int row = uh * 256 + w * 32 + 4 * (lane >> 3);
    const unsigned rstr = isq ? 8192u : 3584u;
    const rsrc_t ru = mk(isq ? pwq : (um ? pwu : pwg), isq ? 512 * 4096 * 2 : 512 * 1792 * 2);
    const unsigned uo = r < 30 ? (unsigned)row * rstr + (unsigned)(ulc * 128 + (lane & 7) * 16) : OOB;
#pragma unroll
    for (int i = 0; i < 4; ++i) wt[i] = __builtin_amdgcn_raw_buffer_load_b128(ru, uo + i * rstr, 0, 0);
#pragma unroll
    for (int b = 0; b < 4; ++b) xq[b] = __builtin_amdgcn_raw_buffer_load_b64(rx, row * 2 + b * 1024, 0, 0);
  }
  SB();
  if (tid == 0) { *(volatile LAS v4u*)(L + L_FLAG) = v4u{0u, 0u, 0u, 0u}; *(volatile LAS v4u*)(L + L_FLAG2) = v4u{0u, 0u, 0u, 0u}; }
  LGKM0(); __builtin_amdgcn_s_barrier(); CB();
  SB();
  const unsigned ko = c * 256 + g * 16, vo = g * 1024 + c * 16;
  v4u kb[NBUF][4], vb[NBUF][4];
  auto issueK = [&](int j) {
    const unsigned so = (unsigned)(j * 8 + w) * 4096u;
#pragma unroll
    for (int i = 0; i < 4; ++i) kb[j % NBUF][i] = __builtin_amdgcn_raw_buffer_load_b128(rk, ko + i * 64, so, AUX_NT);
    SB();
  };
  auto issueV = [&](int j) {
    const unsigned so = (unsigned)(j * 8 + w) * 4096u;
#pragma unroll
    for (int i = 0; i < 4; ++i) vb[j % NBUF][i] = __builtin_amdgcn_raw_buffer_load_b128(rv, vo + i * 256, so, AUX_NT);
    SB();
  };
  auto issue = [&](int j) { issueK(j); issueV(j); };
  const unsigned E = *(const __attribute__((address_space(4))) unsigned*)pws;
  SB();
  issueV(0);
  issueK(0);
  const unsigned tag = E * 0x9E3779B5u + 0x7F4A7C15u;
  const unsigned t8 = tag & 0xffu;
  {
    LAS f32x4* red = (LAS f32x4*)(L + L_RED);
    float v[32];
    rowpair<true>(v, wt[0], wt[1], xq[0].x, xq[1].x, xq[2].x, xq[3].x);
    rowpair<false>(v, wt[2], wt[3], xq[0].y, xq[1].y, xq[2].y, xq[3].y);
    red[w * 64 + lane] = treduce8(v, lane);
    LGKM0();
    const unsigned old = lds_inc(L + L_FLAG + 4, lane);
    CB();
    const f32x4 s = ((red[lane] + red[64 + lane]) + (red[128 + lane] + red[192 + lane])) + ((red[256 + lane] + red[320 + lane]) + (red[384 + lane] + red[448 + lane]));
    const int c64 = (lane & 7) * 8 + ((lane >> 3) & 1) * 4;
    const unsigned po = isq ? WS_QG + (unsigned)((((kvh * 4 + g) * 2 + uh) * 512 + (r >> 1) * 64 + c64) * 4)
                            : WS_GU + (unsigned)((((um * 2 + uh) * 4 + g) * 1792 + ulc * 64 + c64) * 4);
    __builtin_amdgcn_raw_buffer_store_b128(v4u{emb(s[0], t8), emb(s[1], t8), emb(s[2], t8), emb(s[3], t8)}, rws, (old == 7 && r < 30) ? po : OOB, 0, AUX_SC1);
    SB();
    __builtin_amdgcn_s_setprio(0);
    SB();
  }
#pragma unroll
  for (int j = 1; j < QPRE; ++j) issue(j);
  const unsigned qgo = w < 2 ? WS_QG + (unsigned)((kvh * 4 + b_att) * 4096 + w * 1024 + lane * 16) : OOB;
  CB();
  v4u qa = __builtin_amdgcn_raw_buffer_load_b128(rws, qgo, 0, AUX_SC1);
  v4u qb = __builtin_amdgcn_raw_buffer_load_b128(rws, qgo + 2048, 0, AUX_SC1);
  SB();
  issue(QPRE);
  if (w < 2) {
    if (__builtin_amdgcn_ballot_w64((emb_bad(qa, t8) | emb_bad(qb, t8)) != 0u) != 0ull) {
      for (unsigned spin = 0; spin < SPIN_CAP; ++spin) {
        CB();
        qa = __builtin_amdgcn_raw_buffer_load_b128(rws, qgo, 0, AUX_SC1);
        qb = __builtin_amdgcn_raw_buffer_load_b128(rws, qgo + 2048, 0, AUX_SC1);
        if (__builtin_amdgcn_ballot_w64((emb_bad(qa, t8) | emb_bad(qb, t8)) != 0u) == 0ull) break;
      }
    }
    *(LAS v2u*)(L + L_Q + (w * 128 + lane * 2) * 4) = v2u{pk_bf16(unemb(qa.x) + unemb(qb.x), unemb(qa.y) + unemb(qb.y)), pk_bf16(unemb(qa.z) + unemb(qb.z), unemb(qa.w) + unemb(qb.w))};
    LGKM0();
    (void)lds_inc(L + L_FLAG, lane);
  }
  SB();
#pragma unroll
  for (int j = QPRE + 1; j < NBUF; ++j) issue(j);
  auto lds_wait = [&](int off, unsigned n) {
    for (unsigned spin = 0; spin < (SPIN_CAP << 4); ++spin) {
      if (*(volatile LAS unsigned*)(L + off) >= n) break;
      __builtin_amdgcn_s_sleep(1);
    }
    CB();
  };
  lds_wait(L_FLAG, 2u);
  v4u qf[4];
#pragma unroll
  for (int i = 0; i < 4; ++i) qf[i] = *(LAS v4u*)(L + L_Q + (((c & 3) * 64 + 16 * i + 4 * g) * 4));

  f32x4 acc[8];
#pragma unroll
  for (int e = 0; e < 8; ++e) acc[e] = f32x4{0.f, 0.f, 0.f, 0.f};
  float m = -INFINITY, l = 0.f;
#pragma unroll
  for (int j = 0; j < 8; ++j) {
    const int bf = j % NBUF;
    f32x4 sc = {0.f, 0.f, 0.f, 0.f};
#pragma unroll
    for (int i = 0; i < 4; ++i) sc = mfma32(kb[bf][i], qf[i], sc);
    constexpr float SCL = 0.08838834764831845f, L2E = 1.4426950408889634f;
    const unsigned s01 = pk_bf16(sc[0], sc[1]), s23 = pk_bf16(sc[2], sc[3]);
    const unsigned u01 = pk_bf16(bf_lo(s01) * SCL, bf_hi(s01) * SCL), u23 = pk_bf16(bf_lo(s23) * SCL, bf_hi(s23) * SCL);
    const float t0 = bf_lo(u01) * L2E, t1 = bf_hi(u01) * L2E, t2 = bf_lo(u23) * L2E, t3 = bf_hi(u23) * L2E;
    const float cm = xrow_max(fmaxf(fmaxf(t0, t1), fmaxf(t2, t3)));
    const float mn = fmaxf(m, cm);
    const float alpha = __builtin_amdgcn_exp2f(m - mn);
    const float p0 = __builtin_amdgcn_exp2f(t0 - mn), p1 = __builtin_amdgcn_exp2f(t1 - mn), p2 = __builtin_amdgcn_exp2f(t2 - mn), p3 = __builtin_amdgcn_exp2f(t3 - mn);
    l = l * alpha + ((p0 + p1) + (p2 + p3));
    m = mn;
    const v2u pa = {pk_bf16(p0, p1), pk_bf16(p2, p3)};
    if (j > 0) {
      const float a0 = dpp<0x00>(alpha), a1 = dpp<0x55>(alpha), a2 = dpp<0xAA>(alpha), a3 = dpp<0xFF>(alpha);
#pragma unroll
      for (int e = 0; e < 8; ++e) { acc[e][0] *= a0; acc[e][1] *= a1; acc[e][2] *= a2; acc[e][3] *= a3; }
    }
#pragma unroll
    for (int d = 0; d < 4; ++d) {
      const v2u lo = {__builtin_amdgcn_perm(vb[bf][1][d], vb[bf][0][d], 0x05040100u), __builtin_amdgcn_perm(vb[bf][3][d], vb[bf][2][d], 0x05040100u)};
      const v2u hi = {__builtin_amdgcn_perm(vb[bf][1][d], vb[bf][0][d], 0x07060302u), __builtin_amdgcn_perm(vb[bf][3][d], vb[bf][2][d], 0x07060302u)};
      acc[2 * d] = mfma16(pa, lo, acc[2 * d]);
      acc[2 * d + 1] = mfma16(pa, hi, acc[2 * d + 1]);
    }
    if (j == QPRE) asm volatile("" :: "v"(qa), "v"(qb));
    SB();
    if (j + NBUF < 8) issue(j + NBUF);
    if (j + NBUF == 7) { if (lane == 0) (void)__hip_atomic_fetch_add((LAS unsigned*)(L + L_FLAG + 8), 1u, __ATOMIC_RELAXED, __HIP_MEMORY_SCOPE_WORKGROUP); SB(); }
  }
  l = xrow_sum(l);
  if (g == 0) {
    LAS float* mo = (LAS float*)(L + L_MRG) + w * 512 + 8 * c;
#pragma unroll
    for (int h = 0; h < 4; ++h) {
      *(LAS f32x4*)(mo + h * 128) = f32x4{acc[0][h], acc[1][h], acc[2][h], acc[3][h]};
      *(LAS f32x4*)(mo + h * 128 + 4) = f32x4{acc[4][h], acc[5][h], acc[6][h], acc[7][h]};
    }
    if (c < 4) *(LAS f32x2*)(L + L_ML + (c * 8 + w) * 8) = f32x2{m, l};
  }
  LGKM0();
  (void)lds_inc(L + L_FLAG + 12, lane);
  const f32x4 zero = {0.f, 0.f, 0.f, 0.f};
  if (w < 2) {
    lds_wait(L_FLAG + 8, 8u);
    v2u r0[8], r1[8], r2[8];
    v4u bo[2][4], bd[4];
    const unsigned colb = (unsigned)(cq * 128 + w * 64 + 4 * c) * 2u;
    const unsigned wdb = (unsigned)(pidx * 28 + 8 * g) * 1024u + colb;
#pragma unroll
    for (int i = 0; i < 8; ++i) r2[i] = __builtin_amdgcn_raw_buffer_load_b64(rwd, (8 * g + i < 28) ? wdb + i * 1024 : OOB, 0, 0);
    const unsigned wob = (unsigned)(kv2 * 512 + rb * 64 + 8 * g) * 1024u + colb;
#pragma unroll
    for (int i = 0; i < 8; ++i) r0[i] = __builtin_amdgcn_raw_buffer_load_b64(rwo, wob + i * 1024, 0, 0);
#pragma unroll
    for (int i = 0; i < 8; ++i) r1[i] = __builtin_amdgcn_raw_buffer_load_b64(rwo, wob + 32768 + i * 1024, 0, 0);
    SB();
    unsigned fgo = WS_FG + (unsigned)((((g * 16 + cq * 4 + w * 2 + (c >> 3)) * 64 + pidx) * 32 + (c & 7) * 4) * 4);
    lds_wait(L_FLAG2, 1u);
    const v4u hf = *(LAS v4u*)(L + L_H + ((c >> 2) * 32 + 8 * g) * 2);
    tr8x4(r2, bd);
    f32x4 db[4];
#pragma unroll
    for (int e = 0; e < 4; ++e) db[e] = mfma32(hf, bd[e], zero);
    tr8x4(r0, bo[0]); tr8x4(r1, bo[1]);
#pragma unroll
    for (int e = 0; e < 4; ++e) asm volatile("" : "+v"(bo[0][e]), "+v"(bo[1][e]), "+v"(db[e]));
    asm volatile("" : "+v"(fgo));
    lds_wait(L_FLAG2 + 4, 4u);
    const v4u of0 = *(LAS v4u*)(L + L_O + ((c >> 2) * 64 + 8 * g) * 2), of1 = *(LAS v4u*)(L + L_O + ((c >> 2) * 64 + 32 + 8 * g) * 2);
#pragma unroll
    for (int e = 0; e < 4; ++e) db[e] = mfma32(of0, bo[0][e], db[e]);
#pragma unroll
    for (int e = 0; e < 4; ++e) db[e] = mfma32(of1, bo[1][e], db[e]);
    __builtin_amdgcn_raw_buffer_store_b128(v4u{emb(db[0][0], t8), emb(db[1][0], t8), emb(db[2][0], t8), emb(db[3][0], t8)}, rws, fgo, 0, AUX_SC1);
  } else if (w == 2) {
    const unsigned go = c < 7 ? WS_GU + (unsigned)((g * 1792 + pidx * 28 + c * 4) * 4) : OOB;
    v4u xv[4];
#pragma unroll
    for (int b = 0; b < 4; ++b) xv[b] = __builtin_amdgcn_raw_buffer_load_b128(rx, lane * 16 + b * 1024, 0, 0);
    const v4u gv = __builtin_amdgcn_raw_buffer_load_b128(rgw, lane * 16, 0, 0);
    v4u gq[4];
    for (unsigned spin = 0; spin < SPIN_CAP; ++spin) {
      CB();
#pragma unroll
      for (int i = 0; i < 4; ++i) gq[i] = __builtin_amdgcn_raw_buffer_load_b128(rws, go + i * 28672, 0, AUX_SC1);
      SB();
      if (__builtin_amdgcn_ballot_w64(c >= 7 || (emb_bad(gq[0], t8) | emb_bad(gq[1], t8) | emb_bad(gq[2], t8) | emb_bad(gq[3], t8)) == 0u) == ~0ull) break;
      __builtin_amdgcn_s_sleep(2);
    }
    float a[4];
#pragma unroll
    for (int b = 0; b < 4; ++b) { a[b] = dot2(xv[b].x, gv.x, 0.f); a[b] = dot2(xv[b].y, gv.y, a[b]); a[b] = dot2(xv[b].z, gv.z, a[b]); a[b] = dot2(xv[b].w, gv.w, a[b]); }
    a[0] = swap32_add(a[0], a[2]); a[1] = swap32_add(a[1], a[3]);
    a[0] = swap16_add(a[0], a[1]);
    a[0] += dpp<ROW_ROR + 8>(a[0]); a[0] += dpp<ROW_ROR + 4>(a[0]); a[0] += dpp<ROW_ROR + 2>(a[0]); a[0] += dpp<ROW_ROR + 1>(a[0]);
    const float gt = 1.f / (1.f + __expf(-a[0]));
    if (c == 0) *(LAS float*)(L + L_GATE + g * 4) = gt;
    float hh[4];
#pragma unroll
    for (int i = 0; i < 4; ++i) hh[i] = (1.f - gt) * (unemb(gq[2][i]) + unemb(gq[3][i])) / (1.f + __expf(-(unemb(gq[0][i]) + unemb(gq[1][i]))));
    if (c < 8) *(LAS v2u*)(L + L_H + (g * 16 + c * 2) * 4) = c < 7 ? v2u{pk_bf16(hh[0], hh[1]), pk_bf16(hh[2], hh[3])} : v2u{0u, 0u};
    LGKM0();
    if (lane == 0) *(volatile LAS unsigned*)(L + L_FLAG2) = 1u;
  } else if (w >= 4) {
    const int v = w - 4;
    unsigned pgo = WS_PG + (unsigned)(((kvh * 32 + r) * 512 + v * 128 + lane * 2) * 4);
    unsigned po = WS_PG + (unsigned)(((kv2 * 32 + v * 8 + (lane >> 3)) * 512 + rb * 64 + (lane & 7) * 4) * 4);
    asm volatile("" : "+v"(pgo), "+v"(po));
    lds_wait(L_FLAG + 12, 8u);
    {
      f32x4 ml[4]; f32x2 ov[8];
#pragma unroll
      for (int i = 0; i < 4; ++i) ml[i] = *(LAS f32x4*)(L + L_ML + v * 64 + i * 16);
#pragma unroll
      for (int i = 0; i < 8; ++i) ov[i] = *(LAS f32x2*)(L + L_MRG + (i * 512 + v * 128 + lane * 2) * 4);
      SB();
      const float M = fmaxf(fmaxf(fmaxf(ml[0][0], ml[0][2]), fmaxf(ml[1][0], ml[1][2])), fmaxf(fmaxf(ml[2][0], ml[2][2]), fmaxf(ml[3][0], ml[3][2])));
      float Ls = 0.f; f32x2 o = {0.f, 0.f};
#pragma unroll
      for (int i = 0; i < 4; ++i) {
        const float w0 = __builtin_amdgcn_exp2f(ml[i][0] - M), w1 = __builtin_amdgcn_exp2f(ml[i][2] - M);
        Ls += w0 * ml[i][1] + w1 * ml[i][3]; o += w0 * ov[2 * i] + w1 * ov[2 * i + 1];
      }
      o *= __builtin_amdgcn_rcpf(Ls);
      const unsigned lseu = __float_as_uint(M + __builtin_amdgcn_logf(Ls));
      const unsigned nb2 = lseu >> ((lane & 3) * 8);
      const unsigned t4 = t8 & 0x0fu;
      __builtin_amdgcn_raw_buffer_store_b64(v2u{emb(o.x, t4 | ((nb2 & 15u) << 4)), emb(o.y, t4 | (nb2 & 0xf0u))}, rws, pgo, 0, AUX_SC1);
      SB();
    }
    {
      v4u pl0 = {0, 0, 0, 0}, pl1 = {0, 0, 0, 0};
      bool pok = false;
      lds_wait(L_FLAG2, 1u);
      const float gate = *(LAS float*)(L + L_GATE + v * 4);
      for (unsigned spin = 0; spin < SPIN_CAP; ++spin) {
        CB();
        const bool lv = !pok;
        const unsigned o_ = lv ? po : OOB;
        const v4u a0 = __builtin_amdgcn_raw_buffer_load_b128(rws, o_, 0, AUX_SC1);
        const v4u a1 = __builtin_amdgcn_raw_buffer_load_b128(rws, o_ + 128, 0, AUX_SC1);
        SB();
        const bool take = all8(__builtin_amdgcn_ballot_w64(lv && ((emb_bad(a0, t8) | emb_bad(a1, t8)) & 0x0fu) == 0u), lane);
#pragma unroll
        for (int i = 0; i < 4; ++i) { pl0[i] = take ? a0[i] : pl0[i]; pl1[i] = take ? a1[i] : pl1[i]; }
        pok = pok || take;
        if (__builtin_amdgcn_ballot_w64(!pok) == 0ull) break;
      }
      const unsigned half16 = ((pl0.x >> 4) & 15u) | (pl0.y & 0xf0u) | ((pl0.z & 0xf0u) << 4) | ((pl0.w & 0xf0u) << 8);
      const unsigned other = __builtin_amdgcn_mov_dpp(half16, 0xB1, 0xf, 0xf, true);
      const float lse = __uint_as_float((lane & 1) ? (other | (half16 << 16)) : (half16 | (other << 16)));
      float M = fmaxf(lse, dpp<ROW_ROR + 8>(lse)); M = xrow_max(M);
      const float wt_ = __builtin_amdgcn_exp2f(lse - M);
      float W = wt_; W += dpp<ROW_ROR + 8>(W); W = xrow_sum(W);
      float u[4];
#pragma unroll
      for (int i = 0; i < 4; ++i) u[i] = swap32_add(wt_ * unemb(pl0[i]), wt_ * unemb(pl1[i]));
      u[0] = swap16_add(u[0], u[2]); u[1] = swap16_add(u[1], u[3]);
      u[0] += dpp<ROW_ROR + 8>(u[0]); u[1] += dpp<ROW_ROR + 8>(u[1]);
      const float inv = gate * __builtin_amdgcn_rcpf(W);
      if ((lane & 8) == 0) *(LAS unsigned*)(L + L_O + (v * 64 + (lane >> 5) * 32 + (lane & 7) * 4 + ((lane >> 4) & 1) * 2) * 2) = pk_bf16(u[0] * inv, u[1] * inv);
      LGKM0();
      (void)lds_inc(L + L_FLAG2 + 4, lane);
    }
    if ((r & 3) == 0) {
      const int R = (r >> 2) * 8 + kvh;
      const unsigned fo = WS_FG + (unsigned)(R * 8192 + (lane >> 1) * 128 + v * 32 + (lane & 1) * 16);
      unsigned oo = (lane & 30) == 0 ? (unsigned)(R * 16 + v * 4 + (lane & 1) * 2 + (lane >> 5)) * 4u : OOB;
      asm volatile("" : "+v"(oo));
      v4u pl0 = {0, 0, 0, 0}, pl1 = {0, 0, 0, 0};
      bool ok0 = false, ok1 = false;
      lds_wait(L_FLAG2 + 4, 4u);
      __builtin_amdgcn_s_sleep(20);
      for (unsigned spin = 0; spin < SPIN_CAP; ++spin) {
        CB();
        const bool l0 = !ok0, l1 = !ok1;
        const v4u c0 = __builtin_amdgcn_raw_buffer_load_b128(rws, l0 ? fo : OOB, 0, AUX_SC1);
        const v4u c1 = __builtin_amdgcn_raw_buffer_load_b128(rws, l1 ? fo + 4096 : OOB, 0, AUX_SC1);
        SB();
        const bool t0_ = all2(__builtin_amdgcn_ballot_w64(l0 && emb_bad(c0, t8) == 0u), lane);
        const bool t1_ = all2(__builtin_amdgcn_ballot_w64(l1 && emb_bad(c1, t8) == 0u), lane);
#pragma unroll
        for (int i = 0; i < 4; ++i) { pl0[i] = t0_ ? c0[i] : pl0[i]; pl1[i] = t1_ ? c1[i] : pl1[i]; }
        ok0 = ok0 || t0_; ok1 = ok1 || t1_;
        if (__builtin_amdgcn_ballot_w64(!(ok0 && ok1)) == 0ull) break;
      }
      float v0 = unemb(pl0.x) + unemb(pl1.x), v1 = unemb(pl0.y) + unemb(pl1.y), v2 = unemb(pl0.z) + unemb(pl1.z), v3 = unemb(pl0.w) + unemb(pl1.w);
      v0 = swap32_add(v0, v2); v1 = swap32_add(v1, v3);
      v0 = swap16_add(v0, v0); v1 = swap16_add(v1, v1);
      v0 += dpp<ROW_ROR + 8>(v0); v1 += dpp<ROW_ROR + 8>(v1);
      v0 += dpp<ROW_ROR + 4>(v0); v1 += dpp<ROW_ROR + 4>(v1);
      v0 += dpp<ROW_ROR + 2>(v0); v1 += dpp<ROW_ROR + 2>(v1);
      __builtin_amdgcn_raw_buffer_store_b32(pk_bf16(v0, v1), mk(pout, 4096), oo, 0, 0);
      __builtin_amdgcn_raw_buffer_store_b32(tag, rws, (blk == 0 && tid == 256) ? 0u : OOB, 0, AUX_SC1);
    }
  }
}
}

extern "C" void kernel_launch(void* const* d_in, const int* in_sizes, int n_in, void* d_out, int out_size, void* d_ws,
                              size_t ws_size, hipStream_t stream) {
  fk::fused_kernel<<<256, 512, 0, stream>>>(d_in[0], d_in[1], d_in[2], d_in[3], d_in[4], d_in[6], d_in[7], d_in[8], d_in[9], d_out, d_ws);
}
```
